```python
import torch

def _softmax(x, axis=-1):
    return torch.softmax(x, dim=axis)
def _gelu(x):
    return torch.nn.functional.gelu(x, approximate="tanh")
def _silu(x):
    return torch.nn.functional.silu(x)
def _sigmoid(x):
    return torch.sigmoid(x)
def _layernorm(x, axis=-1, eps=1e-5):
    m = x.mean(dim=axis, keepdim=True)
    v = x.var(dim=axis, keepdim=True, unbiased=False)
    return (x - m) / torch.sqrt(v + eps)
def _scatter_add(tgt, idx, src, axis=0):
    return tgt.clone().index_add_(axis, idx, src)
def _segment_sum(src, seg, n):
    out = torch.zeros((n,) + src.shape[1:], dtype=src.dtype)
    return out.index_add_(0, seg, src)
def _pad(x, pw):
    flat = tuple(v for pair in reversed(pw) for v in pair)
    return torch.nn.functional.pad(x, flat)

kernel_name = 'gated_mix__grouped_kv_mqa__dense_activation_gelu__prompt'

def setup_inputs():

    _w = (lambda a: (lambda t: t.to(torch.bfloat16) if t.is_floating_point() else t)(torch.as_tensor(a)))
    def _A_setup():
        B, S, D, H = 1, 2048, 448, 16
        Dh = D // H
        return {
            "x": _w((lambda *s: torch.randn(*s, dtype=torch.bfloat16) * 0.05)(B, S, D)),
            "wq": _w((lambda *s: torch.randn(*s, dtype=torch.bfloat16) * 1.6)(D, H * Dh)),
            "wk": _w((lambda *s: torch.randn(*s, dtype=torch.bfloat16) * 1.6)(D, Dh)),
            "wv": _w((lambda *s: torch.randn(*s, dtype=torch.bfloat16) * 0.05)(D, Dh)),
            "wo": _w((lambda *s: torch.randn(*s, dtype=torch.bfloat16) * 0.05)(H * Dh, D)),
        }
    _A_in = _A_setup()

    def _B_setup():
        B, S, D, F = 1, 2048, 448, 1792
        return {
            "x": _w((lambda *s: torch.randn(*s, dtype=torch.bfloat16) * 0.05)(B, S, D)),
            "w1": _w((lambda *s: torch.randn(*s, dtype=torch.bfloat16) * 3.2)(D, F) * (D ** -0.5)),
            "b1": _w((lambda *s: torch.randn(*s, dtype=torch.bfloat16) * 3.2)(F) * 0.01),
            "w2": _w((lambda *s: torch.randn(*s, dtype=torch.bfloat16) * 0.4)(F, D) * (F ** -0.5)),
            "b2": _w((lambda *s: torch.randn(*s, dtype=torch.bfloat16) * 0.8)(D) * 0.01),
        }
    _B_in = _B_setup()

    return {
        "A_x": _A_in["x"],
        "A_wq": _A_in["wq"],
        "A_wk": _A_in["wk"],
        "A_wv": _A_in["wv"],
        "A_wo": _A_in["wo"],
        "B_x": _B_in["x"],
        "B_w1": _B_in["w1"],
        "B_b1": _B_in["b1"],
        "B_w2": _B_in["w2"],
        "B_b2": _B_in["b2"],
        "gate_w": _w((lambda *s: torch.randn(*s, dtype=torch.bfloat16) * 0.05)(448) * 16.0),
    }

def reference(A_x, A_wq, A_wk, A_wv, A_wo, B_x, B_w1, B_b1, B_w2, B_b2, gate_w):
    def _A_ref(x, wq, wk, wv, wo):
        B, S, D = x.shape
        H = 16
        Dh = D // H
        q = (lambda x, *d: x.permute(*d))((x @ wq).reshape(B, S, H, Dh), 0, 2, 1, 3)
        k = x @ wk
        v = x @ wv
        scores = torch.einsum("bhqd,bkd->bhqk", q, k) * (Dh ** -0.5)
        p = _softmax(scores, axis=-1)
        o = torch.einsum("bhqk,bkd->bhqd", p, v)
        return (lambda x, *d: x.permute(*d))(o, 0, 2, 1, 3).reshape(B, S, H * Dh) @ wo

    def _B_ref(x, w1, b1, w2, b2):
        h = torch.einsum("bsd,df->bsf", x, w1) + b1
        h = _gelu(h)
        return torch.einsum("bsf,fd->bsd", h, w2) + b2

    x = A_x
    g = _sigmoid((x * gate_w).sum(-1, keepdims=True))
    a = _A_ref(x, A_wq, A_wk, A_wv, A_wo)
    b = _B_ref(x, B_w1, B_b1, B_w2, B_b2)
    return g * a + (1 - g) * b
```

```cpp
#include <hip/hip_runtime.h>
#include <hip/hip_bf16.h>
typedef __hip_bfloat16 bf16;
__device__ __forceinline__ float b2f(bf16 v){ return __bfloat162float(v); }
__device__ __forceinline__ float rb(float v){ return __bfloat162float(__float2bfloat16(v)); }

constexpr int S=2048, D=448, H=16, DH=28, F=1792;

__global__ void gemm_naive(const bf16* A, const bf16* B, bf16* C, int M, int N, int K, const bf16* bias, int act){
  int n = blockIdx.x*blockDim.x+threadIdx.x; int m = blockIdx.y;
  if(n>=N) return;
  float acc=0;
  for(int k=0;k<K;k++) acc += b2f(A[m*K+k])*b2f(B[k*N+n]);
  acc = rb(acc);
  if(bias){ acc = rb(acc + b2f(bias[n])); }
  if(act){ float x=acc; float t = tanhf(0.7978845608028654f*(x+0.044715f*x*x*x)); acc = 0.5f*x*(1.f+t);}
  C[m*N+n] = __float2bfloat16(acc);
}
__global__ void attn_naive(const bf16* q, const bf16* k, const bf16* v, bf16* o){
  int t = blockIdx.x, h = blockIdx.y;
  __shared__ float sc[S]; __shared__ float red[256];
  float qv[DH];
  for(int d=0;d<DH;d++) qv[d]=b2f(q[t*D+h*DH+d]);
  float mx=-1e30f;
  for(int j=threadIdx.x;j<S;j+=256){ float a=0; for(int d=0;d<DH;d++) a+=qv[d]*b2f(k[j*DH+d]); a = rb(rb(a)*rb(0.1889822365046136f)); sc[j]=a; mx=fmaxf(mx,a);}
  red[threadIdx.x]=mx; __syncthreads();
  for(int s=128;s>0;s>>=1){ if(threadIdx.x<s) red[threadIdx.x]=fmaxf(red[threadIdx.x],red[threadIdx.x+s]); __syncthreads(); }
  mx=red[0]; __syncthreads();
  float sum=0;
  for(int j=threadIdx.x;j<S;j+=256){ float e=__expf(sc[j]-mx); sc[j]=e; sum+=e; }
  red[threadIdx.x]=sum; __syncthreads();
  for(int s=128;s>0;s>>=1){ if(threadIdx.x<s) red[threadIdx.x]+=red[threadIdx.x+s]; __syncthreads(); }
  sum=red[0]; __syncthreads();
  if(threadIdx.x<DH){ float a=0; for(int j=0;j<S;j++) a+= rb(sc[j]/sum)*b2f(v[j*DH+threadIdx.x]); o[t*D+h*DH+threadIdx.x]=__float2bfloat16(a);}
}
__global__ void final_k(const bf16* x, const bf16* gw, const bf16* a, const bf16* b, bf16* out){
  int t=blockIdx.x; __shared__ float red[512];
  int i=threadIdx.x; float v = i<D ? rb(b2f(x[t*D+i])*b2f(gw[i])) : 0.f;
  red[i]=v; __syncthreads();
  for(int s=256;s>0;s>>=1){ if(i<s) red[i]+=red[i+s]; __syncthreads(); }
  float g = rb(red[0]); g = rb(1.f/(1.f+__expf(-g)));
  if(i<D) out[t*D+i]=__float2bfloat16(rb(g*b2f(a[t*D+i])) + rb(rb(1.f-g)*b2f(b[t*D+i])));
}
extern "C" void kernel_launch(void* const* d_in, const int* in_sizes, int n_in, void* d_out, int out_size, void* d_ws, size_t ws_size, hipStream_t stream) {
  const bf16* x=(const bf16*)d_in[0]; const bf16* wq=(const bf16*)d_in[1]; const bf16* wk=(const bf16*)d_in[2]; const bf16* wv=(const bf16*)d_in[3]; const bf16* wo=(const bf16*)d_in[4];
  const bf16* w1=(const bf16*)d_in[6]; const bf16* b1=(const bf16*)d_in[7]; const bf16* w2=(const bf16*)d_in[8]; const bf16* b2=(const bf16*)d_in[9]; const bf16* gw=(const bf16*)d_in[10];
  bf16* ws=(bf16*)d_ws;
  bf16* q=ws; bf16* k=q+S*D; bf16* v=k+S*DH; bf16* o=v+S*DH; bf16* a=o+S*D; bf16* h=a+S*D; bf16* b=h+S*F;
  gemm_naive<<<dim3(2,S),256,0,stream>>>(x,wq,q,S,D,D,nullptr,0);
  gemm_naive<<<dim3(1,S),64,0,stream>>>(x,wk,k,S,DH,D,nullptr,0);
  gemm_naive<<<dim3(1,S),64,0,stream>>>(x,wv,v,S,DH,D,nullptr,0);
  attn_naive<<<dim3(S,H),256,0,stream>>>(q,k,v,o);
  gemm_naive<<<dim3(2,S),256,0,stream>>>(o,wo,a,S,D,D,nullptr,0);
  gemm_naive<<<dim3(7,S),256,0,stream>>>(x,w1,h,S,F,D,b1,1);
  gemm_naive<<<dim3(2,S),256,0,stream>>>(h,w2,b,S,D,F,b2,0);
  final_k<<<S,512,0,stream>>>(x,gw,a,b,(bf16*)d_out);
}
```

```cpp
#include <hip/hip_runtime.h>
#include <hip/hip_bf16.h>
typedef __hip_bfloat16 bf16;
__device__ __forceinline__ float b2f(bf16 v){ return __bfloat162float(v); }
__device__ __forceinline__ float b2f(float v){ return v; }
constexpr int S=2048, D=448, H=16, DH=28, F=1792;

template<class TA>
__global__ void gemm_naive(const TA* A, const bf16* B, float* C, int M, int N, int K, const bf16* bias, int act){
  int n = blockIdx.x*blockDim.x+threadIdx.x; int m = blockIdx.y;
  if(n>=N) return;
  float acc=0;
  for(int k=0;k<K;k++) acc += b2f(A[m*K+k])*b2f(B[k*N+n]);
  if(bias){ acc = acc + b2f(bias[n]); }
  if(act){ float x=acc; float t = tanhf(0.7978845608028654f*(x+0.044715f*x*x*x)); acc = 0.5f*x*(1.f+t);}
  C[m*N+n] = acc;
}
__global__ void attn_naive(const float* q, const float* k, const float* v, float* o){
  int t = blockIdx.x, h = blockIdx.y;
  __shared__ float sc[S]; __shared__ float red[256];
  float qv[DH];
  for(int d=0;d<DH;d++) qv[d]=b2f(q[t*D+h*DH+d]);
  float mx=-1e30f;
  for(int j=threadIdx.x;j<S;j+=256){ float a=0; for(int d=0;d<DH;d++) a+=qv[d]*b2f(k[j*DH+d]); a = a*0.1889822365046136f; sc[j]=a; mx=fmaxf(mx,a);}
  red[threadIdx.x]=mx; __syncthreads();
  for(int s=128;s>0;s>>=1){ if(threadIdx.x<s) red[threadIdx.x]=fmaxf(red[threadIdx.x],red[threadIdx.x+s]); __syncthreads(); }
  mx=red[0]; __syncthreads();
  float sum=0;
  for(int j=threadIdx.x;j<S;j+=256){ float e=__expf(sc[j]-mx); sc[j]=e; sum+=e; }
  red[threadIdx.x]=sum; __syncthreads();
  for(int s=128;s>0;s>>=1){ if(threadIdx.x<s) red[threadIdx.x]+=red[threadIdx.x+s]; __syncthreads(); }
  sum=red[0]; __syncthreads();
  if(threadIdx.x<DH){ float a=0; for(int j=0;j<S;j++) a+= (sc[j]/sum)*b2f(v[j*DH+threadIdx.x]); o[t*D+h*DH+threadIdx.x]=a;}
}
__global__ void final_k(const bf16* x, const bf16* gw, const float* a, const float* b, bf16* out){
  int t=blockIdx.x; __shared__ float red[512];
  int i=threadIdx.x; float v = i<D ? (b2f(x[t*D+i])*b2f(gw[i])) : 0.f;
  red[i]=v; __syncthreads();
  for(int s=256;s>0;s>>=1){ if(i<s) red[i]+=red[i+s]; __syncthreads(); }
  float g = red[0]; g = (1.f/(1.f+__expf(-g)));
  if(i<D) out[t*D+i]=__float2bfloat16(g*a[t*D+i] + (1.f-g)*b[t*D+i]);
}
extern "C" void kernel_launch(void* const* d_in, const int* in_sizes, int n_in, void* d_out, int out_size, void* d_ws, size_t ws_size, hipStream_t stream) {
  const bf16* x=(const bf16*)d_in[0]; const bf16* wq=(const bf16*)d_in[1]; const bf16* wk=(const bf16*)d_in[2]; const bf16* wv=(const bf16*)d_in[3]; const bf16* wo=(const bf16*)d_in[4];
  const bf16* w1=(const bf16*)d_in[6]; const bf16* b1=(const bf16*)d_in[7]; const bf16* w2=(const bf16*)d_in[8]; const bf16* b2=(const bf16*)d_in[9]; const bf16* gw=(const bf16*)d_in[10];
  float* ws=(float*)d_ws;
  float* q=ws; float* k=q+S*D; float* v=k+S*DH; float* o=v+S*DH; float* a=o+S*D; float* h=a+S*D; float* b=h+S*F;
  gemm_naive<bf16><<<dim3(2,S),256,0,stream>>>(x,wq,q,S,D,D,nullptr,0);
  gemm_naive<bf16><<<dim3(1,S),64,0,stream>>>(x,wk,k,S,DH,D,nullptr,0);
  gemm_naive<bf16><<<dim3(1,S),64,0,stream>>>(x,wv,v,S,DH,D,nullptr,0);
  attn_naive<<<dim3(S,H),256,0,stream>>>(q,k,v,o);
  gemm_naive<float><<<dim3(2,S),256,0,stream>>>(o,wo,a,S,D,D,nullptr,0);
  gemm_naive<bf16><<<dim3(7,S),256,0,stream>>>(x,w1,h,S,F,D,b1,1);
  gemm_naive<float><<<dim3(2,S),256,0,stream>>>(h,w2,b,S,D,F,b2,0);
  final_k<<<S,512,0,stream>>>(x,gw,a,b,(bf16*)d_out);
}
```

```cpp
#include <hip/hip_runtime.h>
#include <hip/hip_bf16.h>
#include <stdint.h>

typedef __attribute__((ext_vector_type(8))) __bf16 bf16x8;
typedef __attribute__((ext_vector_type(4))) __bf16 bf16x4;
typedef __attribute__((ext_vector_type(2))) __bf16 bf16x2;
typedef __attribute__((ext_vector_type(16))) float f32x16;
typedef __attribute__((ext_vector_type(4))) float f32x4;
typedef __attribute__((ext_vector_type(2))) float f32x2;
typedef __attribute__((ext_vector_type(4))) short s16x4;
typedef __attribute__((ext_vector_type(8))) short s16x8;
typedef __attribute__((ext_vector_type(4))) unsigned u32x4;
typedef __attribute__((ext_vector_type(2))) unsigned u32x2;
#define LAS __attribute__((address_space(3)))
#define GAS __attribute__((address_space(1)))
typedef __amdgpu_buffer_rsrc_t srd_t;
typedef LAS char* ldsp;

constexpr int S = 2048, D = 448, NH = 16, DH = 28, F = 1792;
constexpr size_t WS_KV = 0;
constexpr size_t WS_Q  = WS_KV + 64 * 4 * 1024;
constexpr size_t WS_H  = WS_Q + 16 * 64 * 2 * 1024;
constexpr size_t WS_O  = WS_H + 64 * 112 * 1024;
constexpr size_t WS_G  = WS_O + 64 * 32 * 1024;
constexpr size_t WS_QN = WS_G + 2048 * 4;
constexpr size_t WS_KN = WS_QN + 16 * 64 * 4;
constexpr size_t WS_VM = WS_KN + 64 * 4;
constexpr size_t WS_B  = WS_VM + 64 * 4;
constexpr size_t WS_PROF = 24u << 20;

#define SRD_W3 0x00020000
__device__ __forceinline__ srd_t mk_srd(const void* p, unsigned bytes) {
  return __builtin_amdgcn_make_buffer_rsrc((void*)p, (short)0, (int)bytes, SRD_W3);
}
__device__ __forceinline__ void dma16(unsigned m0v, unsigned voff, srd_t srd, unsigned soff) {
  asm volatile("s_mov_b32 m0, %0\n\ts_nop 0\n\tbuffer_load_dwordx4 %1, %2, %3 offen lds"
               :: "s"(m0v), "v"(voff), "s"(srd), "s"(soff) : "memory");
}
__device__ __forceinline__ void dma4(unsigned m0v, unsigned voff, srd_t srd, unsigned soff) {
  asm volatile("s_mov_b32 m0, %0\n\ts_nop 0\n\tbuffer_load_dword %1, %2, %3 offen lds"
               :: "s"(m0v), "v"(voff), "s"(srd), "s"(soff) : "memory");
}
template <int N> __device__ __forceinline__ void wait_vm() { asm volatile("s_waitcnt vmcnt(%0)" :: "n"(N) : "memory"); }
__device__ __forceinline__ void bar() { asm volatile("s_barrier" ::: "memory"); }
__device__ __forceinline__ bf16x8 rd128(ldsp p) { return *(LAS bf16x8*)p; }
__device__ __forceinline__ bf16x8 rdtr(ldsp p0, ldsp p1) {
  s16x4 a = __builtin_amdgcn_ds_read_tr16_b64_v4i16((LAS s16x4*)p0);
  s16x4 b = __builtin_amdgcn_ds_read_tr16_b64_v4i16((LAS s16x4*)p1);
  s16x8 r = __builtin_shufflevector(a, b, 0, 1, 2, 3, 4, 5, 6, 7);
  return __builtin_bit_cast(bf16x8, r);
}
__device__ __forceinline__ f32x16 mfma(bf16x8 a, bf16x8 b, f32x16 c) {
  return __builtin_amdgcn_mfma_f32_32x32x16_bf16(a, b, c, 0, 0, 0);
}
__device__ __forceinline__ unsigned pk(float lo, float hi) {
  f32x2 v = {lo, hi};
  bf16x2 r = __builtin_convertvector(v, bf16x2);
  return __builtin_bit_cast(unsigned, r);
}
__device__ __forceinline__ float bflo(unsigned u) { return __uint_as_float(u << 16); }
__device__ __forceinline__ float bfhi(unsigned u) { return __uint_as_float(u & 0xffff0000u); }
template <int CTRL> __device__ __forceinline__ float dpp_f(float v) {
  return __int_as_float(__builtin_amdgcn_update_dpp(0, __float_as_int(v), CTRL, 0xf, 0xf, true));
}
__device__ __forceinline__ float wave_max_nn(float v) {
  v = fmaxf(v, dpp_f<0xB1>(v));
  v = fmaxf(v, dpp_f<0x4E>(v));
  v = fmaxf(v, dpp_f<0x141>(v));
  v = fmaxf(v, dpp_f<0x140>(v));
  unsigned a = __builtin_amdgcn_readlane(__float_as_uint(v), 0), b = __builtin_amdgcn_readlane(__float_as_uint(v), 16);
  unsigned c = __builtin_amdgcn_readlane(__float_as_uint(v), 32), d = __builtin_amdgcn_readlane(__float_as_uint(v), 48);
  a = a > b ? a : b; c = c > d ? c : d; a = a > c ? a : c;
  return __uint_as_float(a);
}
__device__ __forceinline__ float half_sum(float s) {
  auto r = __builtin_amdgcn_permlane32_swap(__float_as_uint(s), __float_as_uint(s), false, false);
  return __uint_as_float(r[0]) + __uint_as_float(r[1]);
}

#define STAMP(ws, kern, idx) do {} while (0)
#define PROF_ARG
#define PROF_PASS
#define PROF_INIT
#define PROF_DUMP(ws) do {} while (0)

__device__ __forceinline__ float gelu_f(float x, float c1, float c2) {
  float t = x * x;
  float p = __builtin_fmaf(t, c2, c1);
  float e = __builtin_amdgcn_exp2f(x * p);
  return x * __builtin_amdgcn_rcpf(1.0f + e);
}

#define SB() __builtin_amdgcn_sched_barrier(0)
#define PIN(x) asm volatile("" : "+v"(x))
__device__ __forceinline__ void ld16_sc1(u32x4& d, unsigned voff, srd_t srd, unsigned soff) {
  asm volatile("buffer_load_dwordx4 %0, %1, %2, %3 offen sc1" : "=v"(d) : "v"(voff), "s"(srd), "s"(soff) : "memory");
}
template <int OFF> __device__ __forceinline__ void ld16o_sc1(u32x4& d, unsigned voff, srd_t srd, unsigned soff) {
  asm volatile("buffer_load_dwordx4 %0, %1, %2, %3 offen offset:%4 sc1" : "=v"(d) : "v"(voff), "s"(srd), "s"(soff), "n"(OFF) : "memory");
}
struct KVT { u32x4 k0, k1, v0, v1; };
__device__ __forceinline__ void kv_issue(KVT& t, unsigned voff, srd_t srd, unsigned soff) {
  ld16o_sc1<0>(t.k0, voff, srd, soff); ld16o_sc1<1024>(t.k1, voff, srd, soff);
  ld16o_sc1<2048>(t.v0, voff, srd, soff); ld16o_sc1<3072>(t.v1, voff, srd, soff);
}
template <int N> __device__ __forceinline__ void kv_wait(KVT& t) {
  asm volatile("s_waitcnt vmcnt(%4)" : "+v"(t.k0), "+v"(t.k1), "+v"(t.v0), "+v"(t.v1) : "n"(N) : "memory");
}
__device__ __forceinline__ bf16x8 asb(u32x4 v) { return __builtin_bit_cast(bf16x8, v); }
__device__ __forceinline__ f32x16 qk(const KVT& t, const bf16x8 (&q)[2]) {
  f32x16 z;
#pragma unroll
  for (int r = 0; r < 16; r++) z[r] = 0.f;
  z = mfma(asb(t.k0), q[0], z);
  return mfma(asb(t.k1), q[1], z);
}
struct Carry { float e[4]; unsigned p0, p1; };
#define EX(x) __builtin_amdgcn_exp2f(x)
template <bool has_prev, bool has_next>
__device__ __forceinline__ void attn_unit(f32x16& s, f32x16& o, f32x16& oprev, Carry& c, const u32x4& v0, const u32x4& v1prev,
                                          const u32x4& k0n, const u32x4& k1n, const bf16x8 (&qn)[2]) {
  f32x16 sn;
#pragma unroll
  for (int r = 0; r < 16; r++) sn[r] = 0.f;
  if (has_next) { sn = mfma(asb(k0n), qn[0], sn); PIN(sn); }
  SB();
  u32x4 pp;
  if (has_prev) { pp[0] = c.p0; pp[1] = c.p1; pp[2] = pk(c.e[0], c.e[1]); pp[3] = pk(c.e[2], c.e[3]); }
  float e0 = EX(s[0]), e1 = EX(s[1]), e2 = EX(s[2]), e3 = EX(s[3]);
  PIN(e0); PIN(e1); PIN(e2); PIN(e3);
  if (has_prev) PIN(pp);
  SB();
  if (has_prev) { oprev = mfma(asb(v1prev), asb(pp), oprev); PIN(oprev); }
  SB();
  float e4 = EX(s[4]), e5 = EX(s[5]), e6 = EX(s[6]), e7 = EX(s[7]);
  u32x4 p;
  p[0] = pk(e0, e1); p[1] = pk(e2, e3);
  PIN(e4); PIN(e5); PIN(e6); PIN(e7); PIN(p[0]); PIN(p[1]);
  SB();
  if (has_next) { sn = mfma(asb(k1n), qn[1], sn); PIN(sn); }
  SB();
  p[2] = pk(e4, e5); p[3] = pk(e6, e7);
  float e8 = EX(s[8]), e9 = EX(s[9]), e10 = EX(s[10]), e11 = EX(s[11]);
  PIN(p); PIN(e8); PIN(e9); PIN(e10); PIN(e11);
  SB();
  o = mfma(asb(v0), asb(p), o); PIN(o);
  SB();
  c.e[0] = EX(s[12]); c.e[1] = EX(s[13]); c.e[2] = EX(s[14]); c.e[3] = EX(s[15]);
  c.p0 = pk(e8, e9); c.p1 = pk(e10, e11);
  PIN(c.e[0]); PIN(c.e[1]); PIN(c.e[2]); PIN(c.e[3]); PIN(c.p0); PIN(c.p1);
  s = sn;
  SB();
}
template <int MODE>
__device__ __forceinline__ void attn_block(KVT (&kv)[4], f32x16& s, f32x16 (&o)[4], Carry& c, const bf16x8 (&q)[4][2], unsigned voff, srd_t srd,
                                           unsigned soff) {
#pragma unroll
  for (int t = 0; t < 4; t++) {
    KVT& cur = kv[t]; KVT& prv = kv[(t + 3) & 3]; KVT& nxt = kv[(t + 1) & 3];
    if (MODE == 1 && t == 0) attn_unit<false, true>(s, o[0], o[3], c, cur.v0, prv.v1, cur.k0, cur.k1, q[1]);
    else attn_unit<true, true>(s, o[0], o[3], c, cur.v0, prv.v1, cur.k0, cur.k1, q[1]);
    if (MODE != 2 || t == 0) kv_issue(prv, voff, srd, soff + (t + 3) * 4096);
    SB();
    attn_unit<true, true>(s, o[1], o[0], c, cur.v0, cur.v1, cur.k0, cur.k1, q[2]);
    attn_unit<true, true>(s, o[2], o[1], c, cur.v0, cur.v1, cur.k0, cur.k1, q[3]);
    if (MODE != 2 || t == 0) kv_wait<8>(nxt);
    else if (t == 1) kv_wait<4>(nxt);
    else if (t == 2) kv_wait<0>(nxt);
    SB();
    if (MODE == 2 && t == 3) attn_unit<true, false>(s, o[3], o[2], c, cur.v0, cur.v1, cur.k0, cur.k1, q[0]);
    else attn_unit<true, true>(s, o[3], o[2], c, cur.v0, cur.v1, nxt.k0, nxt.k1, q[0]);
  }
  if (MODE == 2) {
    u32x4 pp = {c.p0, c.p1, pk(c.e[0], c.e[1]), pk(c.e[2], c.e[3])};
    o[3] = mfma(asb(kv[3].v1), asb(pp), o[3]);
  }
}
__device__ __forceinline__ void attn_fast(f32x16 (&o)[4], const bf16x8 (&q)[4][2], srd_t srd, unsigned soff0, int l) {
  const unsigned voff = l * 16;
  KVT kv[4];
#pragma unroll
  for (int t = 0; t < 3; t++) kv_issue(kv[t], voff, srd, soff0 + t * 4096);
  kv[3].v1 = kv[3].v0 = kv[3].k0 = kv[3].k1 = u32x4{0, 0, 0, 0};
  kv_wait<8>(kv[0]);
  f32x16 s = qk(kv[0], q[0]);
  Carry c;
  attn_block<1>(kv, s, o, c, q, voff, srd, soff0);
#pragma unroll 1
  for (int it = 1; it < 3; it++) attn_block<0>(kv, s, o, c, q, voff, srd, soff0 + it * 16384);
  attn_block<2>(kv, s, o, c, q, voff, srd, soff0 + 3 * 16384);
}
__device__ __forceinline__ void attn_safe(f32x16 (&o)[4], float (&m)[4], const bf16x8 (&q)[4][2], srd_t srd, unsigned soff0, int l) {
  const unsigned voff = l * 16;
#pragma unroll
  for (int u = 0; u < 4; u++) m[u] = -3.0e38f;
#pragma unroll 1
  for (int t = 0; t < 16; t++) {
    KVT kv;
    kv_issue(kv, voff, srd, soff0 + t * 4096);
    kv_wait<0>(kv);
#pragma unroll
    for (int u = 0; u < 4; u++) {
      f32x16 s = qk(kv, q[u]);
      float mx = s[0];
#pragma unroll
      for (int r = 1; r < 16; r++) mx = fmaxf(mx, s[r]);
      auto sw = __builtin_amdgcn_permlane32_swap(__float_as_uint(mx), __float_as_uint(mx), false, false);
      mx = fmaxf(__uint_as_float(sw[0]), __uint_as_float(sw[1]));
      const float mn = fmaxf(m[u], mx);
      const float al = __builtin_amdgcn_exp2f(m[u] - mn);
      m[u] = mn;
      u32x4 p0, p1;
#pragma unroll
      for (int i = 0; i < 4; i++) {
        p0[i] = pk(__builtin_amdgcn_exp2f(s[2 * i] - mn), __builtin_amdgcn_exp2f(s[2 * i + 1] - mn));
        p1[i] = pk(__builtin_amdgcn_exp2f(s[8 + 2 * i] - mn), __builtin_amdgcn_exp2f(s[8 + 2 * i + 1] - mn));
      }
#pragma unroll
      for (int r = 0; r < 16; r++) o[u][r] *= al;
      o[u] = mfma(asb(kv.v0), asb(p0), o[u]);
      o[u] = mfma(asb(kv.v1), asb(p1), o[u]);
    }
  }
}

__device__ __forceinline__ void grp_arrive(ldsp cnt) {
  asm volatile("s_waitcnt lgkmcnt(0)" ::: "memory");
  if ((threadIdx.x & 63) == 0) __hip_atomic_fetch_add((LAS unsigned*)cnt, 1u, __ATOMIC_RELAXED, __HIP_MEMORY_SCOPE_WORKGROUP);
}
__device__ __forceinline__ void grp_wait(ldsp cnt, unsigned target) {
  while (__builtin_amdgcn_readfirstlane(*(volatile LAS unsigned*)cnt) < target) __builtin_amdgcn_s_sleep(1);
  asm volatile("" ::: "memory");
}

constexpr size_t WS_FL = WS_B;
constexpr unsigned FL_KV = 256, FL_H = 2048, FL_O = 4096;
constexpr int L_X = 0, L_WQ = 57344, L_WKV = 114688, L_R0 = 140288, L_MISC = L_R0 + 18432;
constexpr int L_B1 = L_MISC, L_B2 = L_B1 + 1024, L_GATE = L_B2 + 1024, L_QN = L_GATE + 256, L_CNT = L_QN + 64;
constexpr int L_TOTAL = 163840;
constexpr int C0 = L_CNT, C1 = L_CNT + 4, C2 = L_CNT + 8, C3 = L_CNT + 12, CH = L_CNT + 16, CA = L_CNT + 20, CO = L_CNT + 24, CF = L_CNT + 28, CX = L_CNT + 32;
constexpr int L_RED = L_X, L_M = L_X + 49152;
constexpr unsigned SPIN_LIMIT = 100000;
constexpr int AUX_SC1 = 16;
#define FPRIO 1
#define F2PRIO 0

__device__ __forceinline__ void st16_sc1(u32x4 v, srd_t srd, unsigned voff, unsigned soff) {
  __builtin_amdgcn_raw_buffer_store_b128(v, srd, voff + soff, 0, AUX_SC1);
}
__device__ __forceinline__ void lgkm0() { asm volatile("s_waitcnt lgkmcnt(0)" ::: "memory"); }
__device__ __forceinline__ void cnt_wait(ldsp p, unsigned target) {
#pragma unroll 1
  for (unsigned it = 0; it < (1u << 22); it++) {
    if (__builtin_amdgcn_readfirstlane(*(volatile LAS unsigned*)p) >= target) break;
    __builtin_amdgcn_s_sleep(1);
  }
  asm volatile("" ::: "memory");
}
__device__ __forceinline__ bool cnt_last(ldsp p, unsigned n) {
  unsigned old = 0;
  if ((threadIdx.x & 63) == 0) old = __hip_atomic_fetch_add((LAS unsigned*)p, 1u, __ATOMIC_RELAXED, __HIP_MEMORY_SCOPE_WORKGROUP);
  return __builtin_amdgcn_readfirstlane(old) == n - 1;
}
__device__ __forceinline__ void poll4(srd_t srd, unsigned voff, unsigned tag) {
#pragma unroll 1
  for (unsigned it = 0; it < SPIN_LIMIT; it++) {
    unsigned v;
    asm volatile("buffer_load_dword %0, %1, %2, 0 offen sc1\n\ts_waitcnt vmcnt(0)" : "=v"(v) : "v"(voff), "s"(srd) : "memory");
    if (__builtin_amdgcn_ballot_w64(v != tag) == 0) break;
    __builtin_amdgcn_s_sleep(2);
  }
}
__device__ __forceinline__ u32x2 poll8(srd_t srd, unsigned voff, unsigned tag) {
  u32x2 v;
#pragma unroll 1
  for (unsigned it = 0; it < SPIN_LIMIT; it++) {
    asm volatile("buffer_load_dwordx2 %0, %1, %2, 0 offen sc1\n\ts_waitcnt vmcnt(0)" : "=v"(v) : "v"(voff), "s"(srd) : "memory");
    if (__builtin_amdgcn_ballot_w64(v[1] != tag) == 0) break;
    __builtin_amdgcn_s_sleep(2);
  }
  return v;
}

template <bool SWAP, int WSTEP, int WR2>
__device__ __forceinline__ void proj_chain(f32x16& acc, ldsp lds, unsigned wb, unsigned xl) {
  bf16x8 w[2][7], x[2][7];
  ldsp wp = lds + wb;
  ldsp xp[4];
#pragma unroll
  for (int i = 0; i < 4; i++) xp[i] = lds + (xl ^ (32u * i));
#pragma unroll
  for (int i = 0; i < 7; i++) { w[0][i] = rdtr(wp + i * WSTEP, wp + i * WSTEP + WR2); x[0][i] = rd128(xp[i & 3] + (i >> 2) * 1024); }
  SB();
#pragma unroll
  for (int b = 0; b < 4; b++) {
#pragma unroll
    for (int i = 0; i < 7; i++) {
      const int ks = 7 * b + i;
      acc = SWAP ? mfma(x[b & 1][i], w[b & 1][i], acc) : mfma(w[b & 1][i], x[b & 1][i], acc);
      if (b < 3) {
        const int kn = ks + 7;
        w[(b + 1) & 1][i] = rdtr(wp + kn * WSTEP, wp + kn * WSTEP + WR2); x[(b + 1) & 1][i] = rd128(xp[kn & 3] + (kn >> 2) * 1024);
      }
      SB();
    }
  }
}

template <int I> struct IC { static constexpr int v = I; };
template <int I, int N, class Fn> __device__ __forceinline__ void static_for(Fn&& fn) {
  if constexpr (I < N) { fn(IC<I>{}); static_for<I + 1, N>(fn); }
}
template <int OFF> __device__ __forceinline__ void set_m0(unsigned base) {
  asm volatile("s_add_u32 m0, %0, %1" :: "s"(base), "n"(OFF) : "scc", "memory");
}
__device__ __forceinline__ void dma_go(unsigned voff, srd_t srd, unsigned soff) {
  asm volatile("buffer_load_dwordx4 %0, %1, %2 offen lds" :: "v"(voff), "s"(srd), "s"(soff) : "memory");
}

struct FCtx {
  ldsp lds; unsigned ldsb;
  unsigned ring;
  unsigned ringb;
  unsigned la0, la1;
  unsigned xl;
  unsigned vW1, vW1b, vW2, vW2b;
  srd_t sW1, sW2, sWO;
  unsigned w2o, woo;
  int f, j;
};

template <int NSUB> struct F1Frag { bf16x8 a[NSUB]; bf16x8 b[2]; };
template <int NSUB> __device__ __forceinline__ void f1_issue_w1(const FCtx& c, unsigned ldsdst, int ks) {
  dma16(c.ldsb + ldsdst, c.vW1, c.sW1, ks * (16 * 3584));
  if (NSUB == 2) dma16(c.ldsb + ldsdst + 1024, c.vW1, c.sW1, ks * (16 * 3584) + 8 * 3584);
}
template <int NSUB> __device__ __forceinline__ void f1_load(F1Frag<NSUB>& fr, ldsp a0, ldsp a1, ldsp xp) {
  constexpr int R2 = NSUB == 2 ? 512 : 256;
  fr.a[0] = rdtr(a0, a0 + R2);
  if (NSUB == 2) fr.a[NSUB - 1] = rdtr(a1, a1 + R2);
  fr.b[0] = rd128(xp); fr.b[1] = rd128(xp + 28672);
}
template <int NSUB> __device__ __forceinline__ void f1_mma(const F1Frag<NSUB>& fr, f32x16 (&acc)[2][2]) {
#pragma unroll
  for (int n = 0; n < NSUB; n++) { acc[n][0] = mfma(fr.a[n], fr.b[0], acc[n][0]); acc[n][1] = mfma(fr.a[n], fr.b[1], acc[n][1]); }
}
template <int NSUB, int E>
__device__ __forceinline__ void f1_step(const FCtx& c, F1Frag<NSUB>& cur, F1Frag<NSUB>& nxt, f32x16 (&acc)[2][2], ldsp pa0, ldsp pa1,
                                        const ldsp (&xp)[4], unsigned& soff) {
  constexpr int FIRST = NSUB == 2 ? 3 : 0, R2 = NSUB == 2 ? 512 : 256;
  constexpr int SH = NSUB == 2 ? 0 : 4;
  constexpr int SL = ((E - FIRST + SH) % 7) * 2048, SN = ((E + 1 - FIRST + SH) % 7) * 2048;
  constexpr bool W1 = E < 21;
  constexpr int XO = ((E + 1) >> 2) * 1024;
  wait_vm<NSUB == 2 ? 10 : 5>();
  if (E == 21) soff = c.w2o;
  SB();
  acc[0][0] = mfma(cur.a[0], cur.b[0], acc[0][0]);
  SB();
  if (E < 27) { nxt.a[0] = rdtr(pa0 + SN, pa0 + SN + R2); nxt.b[0] = rd128(xp[(E + 1) & 3] + XO); }
  SB();
  acc[0][1] = mfma(cur.a[0], cur.b[1], acc[0][1]);
  SB();
  if (E < 27) { nxt.b[1] = rd128(xp[(E + 1) & 3] + XO + 28672); if (NSUB == 2) nxt.a[NSUB - 1] = rdtr(pa1 + SN, pa1 + SN + R2); }
  set_m0<SL>(c.ringb);
  SB();
  if (NSUB == 2) { acc[1][0] = mfma(cur.a[NSUB - 1], cur.b[0], acc[1][0]); } else { asm volatile("s_waitcnt lgkmcnt(%0)" :: "n"(E < 27 ? 4 : 0) : "memory"); }
  SB();
  dma_go(W1 ? c.vW1 : c.vW2, W1 ? c.sW1 : c.sW2, soff);
  if (NSUB == 2 || !W1) set_m0<SL + 1024>(c.ringb);
  SB();
  if (NSUB == 2) { acc[1][1] = mfma(cur.a[NSUB - 1], cur.b[1], acc[1][1]); } else if (!W1) asm volatile("s_nop 0");
  SB();
  if (NSUB == 2 || !W1) dma_go(W1 ? c.vW1b : c.vW2b, W1 ? c.sW1 : c.sW2, soff);
  soff += W1 ? 16 * 3584 : 16 * 896;
  SB();
}
template <int NSUB> __device__ __forceinline__ void ffn1(FCtx& c, f32x16 (&acc)[2][2], char* ws PROF_ARG) {
  constexpr int FIRST = NSUB == 2 ? 3 : 0;
  F1Frag<NSUB> f0, f1;
  ldsp xp[4];
#pragma unroll
  for (int i = 0; i < 4; i++) xp[i] = c.lds + (c.xl ^ (32u * i));
  if (NSUB == 2) {
    cnt_wait(c.lds + C0, 4);
    ldsp r0 = c.lds + (L_R0 + c.f * 6144 + c.la0 - c.ring), r1 = c.lds + (L_R0 + c.f * 6144 + c.la1 - c.ring);
    f1_load<NSUB>(f0, r0, r1, xp[0]);
    f1_load<NSUB>(f1, r0 + 2048, r1 + 2048, xp[1]);
    f1_mma<NSUB>(f0, acc);
    f1_load<NSUB>(f0, r0 + 4096, r1 + 4096, xp[2]);
    f1_mma<NSUB>(f1, acc);
    grp_arrive(c.lds + C1);
    STAMP(ws, 0, 11);
    f1_mma<NSUB>(f0, acc);
  }
  cnt_wait(c.lds + C1, 8);
  STAMP(ws, 0, 12);
#pragma unroll
  for (int i = 0; i < 7; i++) f1_issue_w1<NSUB>(c, c.ring + ((i + (NSUB == 2 ? 0 : 4)) % 7) * 2048, FIRST + i);
  unsigned soff = (FIRST + 7) * (16 * 3584);
  ldsp pa0 = c.lds + c.la0, pa1 = c.lds + c.la1;
  wait_vm<NSUB == 2 ? 12 : 6>();
  STAMP(ws, 0, 13);
  f1_load<NSUB>(f0, pa0 + (NSUB == 2 ? 0 : 4 * 2048), pa1, xp[FIRST & 3] + (FIRST >> 2) * 1024);
  static_for<FIRST, 28>([&](auto ic) {
    constexpr int E = decltype(ic)::v;
    if constexpr (((E - FIRST) & 1) == 0) f1_step<NSUB, E>(c, f0, f1, acc, pa0, pa1, xp, soff);
    else f1_step<NSUB, E>(c, f1, f0, acc, pa0, pa1, xp, soff);
  });
}

struct AFr { bf16x8 a[2]; };
struct HB { u32x4 t0, t1; };
template <int N> __device__ __forceinline__ void hb_wait(HB& h) { asm volatile("s_waitcnt vmcnt(%2)" : "+v"(h.t0), "+v"(h.t1) : "n"(N) : "memory"); }
template <int BASE, int S>
__device__ __forceinline__ void f2_step(const FCtx& c, AFr& cur, AFr& nxt, HB& h, f32x16 (&acc)[2][2], ldsp pa0, ldsp pa1, unsigned& soff,
                                        unsigned hv, srd_t sH, unsigned& hs) {
  constexpr int SL = ((BASE + S) % 7) * 2048, SN = ((BASE + S + 1) % 7) * 2048;
  constexpr bool W2 = S < 21;
  constexpr int WAIT = S == 0 ? 6 : S == 1 ? 8 : S == 2 ? 10 : S <= 24 ? 12 : S == 25 ? 10 : S == 26 ? 8 : 6;
  constexpr int T = S - 21;
  hb_wait<WAIT>(h);
  if (!W2) soff = c.woo + (28 * (T >> 1) + 16 * (T & 1)) * 896;
  SB();
  acc[0][0] = mfma(cur.a[0], asb(h.t0), acc[0][0]);
  SB();
  nxt.a[0] = rdtr(pa0 + SN, pa0 + SN + 1024);
  SB();
  acc[0][1] = mfma(cur.a[0], asb(h.t1), acc[0][1]);
  SB();
  nxt.a[1] = rdtr(pa1 + SN, pa1 + SN + 1024);
  set_m0<SL>(c.ringb);
  SB();
  acc[1][0] = mfma(cur.a[1], asb(h.t0), acc[1][0]);
  SB();
  dma_go(c.vW2, W2 ? c.sW2 : c.sWO, soff);
  set_m0<SL + 1024>(c.ringb);
  SB();
  acc[1][1] = mfma(cur.a[1], asb(h.t1), acc[1][1]);
  SB();
  dma_go(c.vW2b, W2 ? c.sW2 : c.sWO, soff);
  if (W2) soff += 16 * 896;
  if (S < 24) { ld16o_sc1<0>(h.t0, hv, sH, hs); ld16o_sc1<0>(h.t1, hv, sH, hs + 112 * 1024); hs += 1024; }
  SB();
}
template <int BASE>
__device__ __forceinline__ void ffn2_tail(const FCtx& c, f32x16 (&acc)[2][2], int g64, int l, unsigned tag, srd_t sWS, srd_t sFL, float& og0,
                                          float& og1, char* ws PROF_ARG) {
  ldsp pa0 = c.lds + c.la0, pa1 = c.lds + c.la1;
  const unsigned hv = l * 16;
  unsigned hs = WS_H + ((2 * g64) * 112 + 28 * c.f) * 1024;
  HB hb[4];
#pragma unroll
  for (int i = 0; i < 4; i++) { ld16_sc1(hb[i].t0, hv, sWS, hs + i * 1024); ld16_sc1(hb[i].t1, hv, sWS, hs + i * 1024 + 112 * 1024); }
  hs += 4096;
#pragma unroll
  for (int n = 0; n < 2; n++)
#pragma unroll
    for (int t = 0; t < 2; t++)
#pragma unroll
      for (int r = 0; r < 16; r++) acc[n][t][r] = 0.f;
  AFr a0, a1;
  a0.a[0] = rdtr(pa0 + (BASE % 7) * 2048, pa0 + (BASE % 7) * 2048 + 1024); a0.a[1] = rdtr(pa1 + (BASE % 7) * 2048, pa1 + (BASE % 7) * 2048 + 1024);
  unsigned soff = c.w2o + 7 * (16 * 896);
  static_for<0, 28>([&](auto ic) {
    constexpr int S = decltype(ic)::v;
    if constexpr ((S & 1) == 0) f2_step<BASE, S>(c, a0, a1, hb[S & 3], acc, pa0, pa1, soff, hv, sWS, hs);
    else f2_step<BASE, S>(c, a1, a0, hb[S & 3], acc, pa0, pa1, soff, hv, sWS, hs);
  });
  STAMP(ws, 0, 6);
  cnt_wait(c.lds + C2, 4);
  og0 = 1.0f - *(LAS float*)(c.lds + L_GATE + (l & 31) * 4); og1 = 1.0f - *(LAS float*)(c.lds + L_GATE + 128 + (l & 31) * 4);
#pragma unroll
  for (int n = 0; n < 2; n++)
#pragma unroll
    for (int r = 0; r < 16; r++) { acc[n][0][r] *= og0; acc[n][1][r] *= og1; }
  lgkm0();
  dma16(c.ringb + ((BASE + 28) % 7) * 2048, c.vW2, c.sWO, c.woo + (28 * 3 + 16) * 896);
  dma16(c.ringb + ((BASE + 28) % 7) * 2048 + 1024, c.vW2b, c.sWO, c.woo + (28 * 3 + 16) * 896);
  poll4(sFL, FL_O + (g64 * 8 + 2 * c.f + (l & 1)) * 4, tag);
  STAMP(ws, 0, 7);
  HB ob[8];
  const unsigned os = WS_O + ((2 * g64) * 32 + 8 * c.f) * 1024;
#pragma unroll
  for (int t = 0; t < 8; t++) { ld16_sc1(ob[t].t0, hv, sWS, os + t * 1024); ld16_sc1(ob[t].t1, hv, sWS, os + t * 1024 + 32 * 1024); }
  static_for<0, 8>([&](auto ic) {
    constexpr int T = decltype(ic)::v;
    constexpr int SN = ((BASE + 28 + T + 1) % 7) * 2048;
    AFr& cur = (T & 1) ? a1 : a0; AFr& nxt = (T & 1) ? a0 : a1;
    hb_wait<14 - 2 * T>(ob[T]);
    if (T < 7) { nxt.a[0] = rdtr(pa0 + SN, pa0 + SN + 1024); nxt.a[1] = rdtr(pa1 + SN, pa1 + SN + 1024); }
    acc[0][0] = mfma(cur.a[0], asb(ob[T].t0), acc[0][0]); acc[0][1] = mfma(cur.a[0], asb(ob[T].t1), acc[0][1]);
    acc[1][0] = mfma(cur.a[1], asb(ob[T].t0), acc[1][0]); acc[1][1] = mfma(cur.a[1], asb(ob[T].t1), acc[1][1]);
    SB();
  });
}

struct Ptrs { const char *x, *wq, *wk, *wv, *wo, *w1, *b1, *w2, *b2, *gw; char *ws, *out; };

__device__ __forceinline__ void zero16(f32x16& a) {
#pragma unroll
  for (int r = 0; r < 16; r++) a[r] = 0.f;
}

__device__ __forceinline__ void f_wave(const Ptrs& P, ldsp lds, unsigned ldsb, int g64, int j, int f, int l, unsigned tag, srd_t sWS, srd_t sFL PROF_ARG) {
  const int h = l >> 5;
  STAMP(ws, 0, 10);
  __builtin_amdgcn_s_setprio(FPRIO);
  FCtx c;
  c.lds = lds; c.ldsb = ldsb; c.f = f; c.j = j;
  c.ring = L_WQ + f * 14336; c.ringb = ldsb + c.ring;
  c.w2o = (unsigned)(28 * f) * (16 * 896); c.woo = (unsigned)(28 * 4 * f) * 896;
  c.sW1 = mk_srd(P.w1, D * F * 2); c.sW2 = mk_srd(P.w2, D * F * 2); c.sWO = mk_srd(P.wo, D * D * 2);
  const unsigned q4 = (l & 15) >> 2, g1 = (l >> 4) & 1;
  const unsigned swz = 16 * ((l & 7) ^ (((l >> 4) & 1) << 2));
  c.vW2 = (l >> 3) * 896 + 128 * j + swz; c.vW2b = c.vW2 + 8 * 896;
  const unsigned cpart = 16 * ((2 * g1 + ((l & 3) >> 1)) ^ (((q4 >> 1) & 1) << 2)) + 8 * (l & 1);
  c.xl = L_X + ((l & 31) >> 3) * 7168 + (l & 7) * 128 + 16 * (h ^ (((l & 31) >> 1) & 7));
  char* ws = P.ws;
  f32x16 acc[2][2];
  if (f == 3) {
    grp_arrive(lds + C1);
    if (j < 4) {
      __builtin_amdgcn_s_setprio(3);
      const int ts = j & 1, t32 = 2 * g64 + ts;
      const unsigned wb = L_WKV + (8 * h + q4) * 56 + 32 * g1 + 8 * (l & 3);
      const unsigned xo = ts * 28672;
      f32x16 a; zero16(a);
      if (j < 2) {
        proj_chain<false, 896, 224>(a, lds, wb, c.xl + xo);
        float k[16]; float ss = 0.f;
#pragma unroll
        for (int r = 0; r < 16; r++) { k[r] = a[r]; if (r >= 12 && h) k[r] = 0.f; ss = __builtin_fmaf(k[r], k[r], ss); }
        u32x4 f0 = {pk(k[0], k[1]), pk(k[2], k[3]), pk(k[4], k[5]), pk(k[6], k[7])};
        u32x4 f1 = {pk(k[8], k[9]), pk(k[10], k[11]), pk(k[12], k[13]), pk(k[14], k[15])};
        st16_sc1(f0, sWS, WS_KV + t32 * 4096 + l * 16, 0); st16_sc1(f1, sWS, WS_KV + t32 * 4096 + 1024 + l * 16, 0);
        const float mx = wave_max_nn(half_sum(ss));
        wait_vm<0>();
        if (l == 0) __builtin_amdgcn_raw_buffer_store_b64(u32x2{__float_as_uint(mx), tag}, sFL, FL_KV + t32 * 16, 0, AUX_SC1);
      } else {
        proj_chain<true, 896, 224>(a, lds, wb, c.xl + xo);
        float v[16]; float mx = 0.f;
        const int d = l & 31;
#pragma unroll
        for (int r = 0; r < 16; r++) { v[r] = d < 28 ? a[r] : 0.f; mx = fmaxf(mx, fabsf(v[r])); if (d == 28) v[r] = 1.0f; }
        u32x4 f0 = {pk(v[0], v[1]), pk(v[2], v[3]), pk(v[4], v[5]), pk(v[6], v[7])};
        u32x4 f1 = {pk(v[8], v[9]), pk(v[10], v[11]), pk(v[12], v[13]), pk(v[14], v[15])};
        st16_sc1(f0, sWS, WS_KV + t32 * 4096 + 2048 + l * 16, 0); st16_sc1(f1, sWS, WS_KV + t32 * 4096 + 3072 + l * 16, 0);
        mx = wave_max_nn(mx);
        wait_vm<0>();
        if (l == 0) __builtin_amdgcn_raw_buffer_store_b64(u32x2{__float_as_uint(mx), tag}, sFL, FL_KV + t32 * 16 + 8, 0, AUX_SC1);
      }
      __builtin_amdgcn_s_setprio(FPRIO);
    }
  }
  STAMP(ws, 0, 2);
  const int nsub = f < 3 ? 2 : 1;
  const int T0 = f < 3 ? 2 * (3 * j + f) : 48 + j;
#pragma unroll
  for (int n = 0; n < 2; n++)
#pragma unroll
    for (int m = 0; m < 4; m++) {
      u32x2 v = *(LAS u32x2*)(lds + L_B1 + f * 256 + (32 * n + 8 * m + 4 * h) * 2);
#pragma unroll
      for (int t = 0; t < 2; t++) {
        acc[n][t][4 * m] = bflo(v[0]); acc[n][t][4 * m + 1] = bfhi(v[0]); acc[n][t][4 * m + 2] = bflo(v[1]); acc[n][t][4 * m + 3] = bfhi(v[1]);
      }
    }
  if (f < 3) {
    c.vW1 = (l >> 3) * 3584 + 64 * T0 + swz; c.vW1b = c.vW1 + 8 * 3584;
    c.la0 = (8 * h + q4) * 128 + cpart; c.la1 = (c.la0 ^ 64) + c.ring; c.la0 += c.ring;
    ffn1<2>(c, acc, ws PROF_PASS);
  } else {
    c.vW1 = (l >> 2) * 3584 + 64 * T0 + 16 * (l & 3); c.vW1b = c.vW1;
    c.la0 = (8 * h + q4) * 64 + 32 * g1 + 8 * (l & 3) + c.ring; c.la1 = c.la0;
    ffn1<1>(c, acc, ws PROF_PASS);
  }
  lgkm0();
  grp_arrive(lds + C3);
  STAMP(ws, 0, 3);
  {
    float c1 = -2.3022082f, c2 = -2.3022082f * 0.044715f;
    asm volatile("" : "+v"(c1), "+v"(c2));
#pragma unroll
    for (int n = 0; n < 2; n++) {
      if (n < nsub) {
#pragma unroll
        for (int t = 0; t < 2; t++) {
          float y[16];
#pragma unroll
          for (int r = 0; r < 16; r++) y[r] = gelu_f(acc[n][t][r], c1, c2);
          u32x4 f0 = {pk(y[0], y[1]), pk(y[2], y[3]), pk(y[4], y[5]), pk(y[6], y[7])};
          u32x4 f1 = {pk(y[8], y[9]), pk(y[10], y[11]), pk(y[12], y[13]), pk(y[14], y[15])};
          const unsigned so = WS_H + ((2 * g64 + t) * 112 + 2 * (T0 + n)) * 1024;
          st16_sc1(f0, sWS, l * 16, so); st16_sc1(f1, sWS, l * 16, so + 1024);
        }
      }
    }
  }
  wait_vm<0>();
  if (cnt_last(lds + CH, 4)) { if (l == 0) __builtin_amdgcn_raw_buffer_store_b32(tag, sFL, FL_H + (g64 * 8 + j) * 4, 0, AUX_SC1); }
  STAMP(ws, 0, 4);
  if (j == 7) {
    if (g64 == 0 && f == 0) {
      poll4(sFL, FL_H + l * 4, tag); poll4(sFL, FL_H + 256 + l * 4, tag); poll4(sFL, FL_H + 512 + l * 4, tag); poll4(sFL, FL_H + 768 + l * 4, tag);
      if (l == 0) __builtin_amdgcn_raw_buffer_store_b32(tag, sFL, 0, 0, AUX_SC1);
    }
    return;
  }
  c.la0 = (4 * h + q4) * 128 + cpart; c.la1 = (c.la0 ^ 64) + c.ring; c.la0 += c.ring;
  __builtin_amdgcn_s_setprio(F2PRIO);
  poll4(sFL, FL_H + (g64 * 8 + (l & 7)) * 4, tag);
  STAMP(ws, 0, 5);
  float og0, og1;
  ffn2_tail<25>(c, acc, g64, l, tag, sWS, sFL, og0, og1, ws PROF_PASS);
  STAMP(ws, 0, 8);
  lgkm0();
#pragma unroll
  for (int qd = 0; qd < 4; qd++) {
    if (qd != f) {
      const unsigned dst = L_WQ + f * 14336 + ((qd - f - 1) & 3) * 4096 + l * 16;
#pragma unroll
      for (int g = 0; g < 4; g++) {
        const f32x16& a = acc[qd >> 1][qd & 1];
        *(LAS f32x4*)(lds + dst + g * 1024) = f32x4{a[4 * g], a[4 * g + 1], a[4 * g + 2], a[4 * g + 3]};
      }
    }
  }
  grp_arrive(lds + CF);
  f32x16 t = acc[0][0];
  if (f == 1) t = acc[0][1]; else if (f == 2) t = acc[1][0]; else if (f == 3) t = acc[1][1];
  const float og = (f & 1) ? og1 : og0;
#pragma unroll
  for (int m = 0; m < 4; m++) {
    u32x2 v = *(LAS u32x2*)(lds + L_B2 + (32 * (f >> 1) + 8 * m + 4 * h) * 2);
    t[4 * m] = __builtin_fmaf(og, bflo(v[0]), t[4 * m]); t[4 * m + 1] = __builtin_fmaf(og, bfhi(v[0]), t[4 * m + 1]);
    t[4 * m + 2] = __builtin_fmaf(og, bflo(v[1]), t[4 * m + 2]); t[4 * m + 3] = __builtin_fmaf(og, bfhi(v[1]), t[4 * m + 3]);
  }
  cnt_wait(lds + CF, 4);
#pragma unroll
  for (int i = 1; i < 4; i++) {
    const int w = (f + i) & 3;
    const unsigned src = L_WQ + w * 14336 + (3 - i) * 4096 + l * 16;
#pragma unroll
    for (int g = 0; g < 4; g++) {
      f32x4 v = *(LAS f32x4*)(lds + src + g * 1024);
#pragma unroll
      for (int e = 0; e < 4; e++) t[4 * g + e] += v[e];
    }
  }
  {
    unsigned p[8];
#pragma unroll
    for (int i = 0; i < 8; i++) p[i] = pk(t[2 * i], t[2 * i + 1]);
    const srd_t sOut = mk_srd(P.out, S * D * 2);
    const unsigned row = (64 * g64 + 32 * (f & 1) + (l & 31)) * 896 + 128 * j + 64 * (f >> 1) + 16 * h;
#pragma unroll
    for (int m = 0; m < 2; m++) {
      auto s0 = __builtin_amdgcn_permlane32_swap(p[4 * m], p[4 * m + 2], false, false);
      auto s1 = __builtin_amdgcn_permlane32_swap(p[4 * m + 1], p[4 * m + 3], false, false);
      u32x4 v = {s0[0], s1[0], s0[1], s1[1]};
      __builtin_amdgcn_raw_buffer_store_b128(v, sOut, row + 32 * m, 0, 0);
    }
  }
  STAMP(ws, 0, 9);
}

__device__ __forceinline__ void a_wave(const Ptrs& P, ldsp lds, unsigned ldsb, int g64, int j, int a, int l, unsigned tag, unsigned gwv, srd_t sWS,
                                       srd_t sFL PROF_ARG) {
  const int h = l >> 5;
  char* ws = P.ws;
  wait_vm<0>();
  asm volatile("" : "+v"(gwv));
  if (l < 56) {
    const unsigned ad = L_WQ + (112 * a + 2 * l) * 128 + 16 * (7 ^ ((l & 1) << 2));
    *(LAS unsigned short*)(lds + ad) = (unsigned short)gwv;
    *(LAS unsigned short*)(lds + ad + 128) = (unsigned short)(gwv >> 16);
  }
  grp_arrive(lds + C0);
  const int hb = a >> 1, ts = a & 1;
  const unsigned q4 = (l & 15) >> 2, g1 = (l >> 4) & 1;
  const unsigned u8 = 7 * hb + 4 * g1 + (l & 3);
  const unsigned wb = L_WQ + (8 * h + q4) * 128 + 16 * ((u8 >> 1) ^ (((q4 >> 1) & 1) << 2)) + 8 * (u8 & 1);
  const unsigned xl = L_X + ts * 28672 + ((l & 31) >> 3) * 7168 + (l & 7) * 128 + 16 * (h ^ (((l & 31) >> 1) & 7));
  cnt_wait(lds + C0, 4);
  cnt_wait(lds + CX, 4);
  STAMP(ws, 0, 2);
  f32x16 acc; zero16(acc);
  proj_chain<false, 2048, 512>(acc, lds, wb, xl);
  PIN(acc);
  grp_arrive(lds + C1);
  STAMP(ws, 0, 14);
  {
    const float qs = 0.18898223650461362f * 1.4426950408889634f;
    const float gt = __builtin_amdgcn_rcpf(1.0f + __builtin_amdgcn_exp2f(acc[12] * -1.4426950408889634f));
    float q[16]; float ss = 0.f;
#pragma unroll
    for (int r = 0; r < 16; r++) { q[r] = acc[r] * qs; if (r >= 12 && h) q[r] = 0.f; ss = __builtin_fmaf(q[r], q[r], ss); }
    u32x4 f0 = {pk(q[0], q[1]), pk(q[2], q[3]), pk(q[4], q[5]), pk(q[6], q[7])};
    u32x4 f1 = {pk(q[8], q[9]), pk(q[10], q[11]), pk(q[12], q[13]), pk(q[14], q[15])};
    const float mx = wave_max_nn(half_sum(ss));
    cnt_wait(lds + C1, 8);
    STAMP(ws, 0, 15);
    *(LAS u32x4*)(lds + L_R0 + a * 2048 + l * 16) = f0; *(LAS u32x4*)(lds + L_R0 + a * 2048 + 1024 + l * 16) = f1;
    if (l == 0) *(LAS float*)(lds + L_QN + a * 4) = mx;
    if (hb && h) *(LAS float*)(lds + L_GATE + (32 * ts + (l & 31)) * 4) = gt;
  }
  grp_arrive(lds + C2);
  cnt_wait(lds + C2, 4);
  bf16x8 q[4][2];
#pragma unroll
  for (int u = 0; u < 4; u++) { q[u][0] = rd128(lds + L_R0 + u * 2048 + l * 16); q[u][1] = rd128(lds + L_R0 + u * 2048 + 1024 + l * 16); }
  const f32x4 qn4 = *(LAS f32x4*)(lds + L_QN);
  const float qn = fmaxf(fmaxf(qn4[0], qn4[1]), fmaxf(qn4[2], qn4[3]));
  const float gate = *(LAS float*)(lds + L_GATE + (32 * (a & 1) + (l & 31)) * 4);
  STAMP(ws, 0, 3);
  const u32x2 fl = poll8(sFL, FL_KV + (32 * a + (l & 31)) * 8, tag);
  STAMP(ws, 0, 4);
  const float st = __uint_as_float(fl[0]);
  const float kn = wave_max_nn((l & 1) ? 0.f : st), vm = wave_max_nn((l & 1) ? st : 0.f);
  const float bound = __builtin_sqrtf(qn * kn) * 1.02f + fabsf(__builtin_amdgcn_logf(vm));
  f32x16 o[4];
#pragma unroll
  for (int u = 0; u < 4; u++) zero16(o[u]);
  float m[4] = {0.f, 0.f, 0.f, 0.f};
  const srd_t sKV = mk_srd(ws + WS_KV, 64 * 4096);
  const bool fast = __builtin_amdgcn_readfirstlane(bound <= 100.0f);
  if (fast) attn_fast(o, q, sKV, a * 16 * 4096, l);
  else attn_safe(o, m, q, sKV, a * 16 * 4096, l);
  STAMP(ws, 0, 5);
  cnt_wait(lds + C3, 4);
#pragma unroll
  for (int u = 0; u < 4; u++) {
    *(LAS float*)(lds + L_M + (a * 4 + u) * 256 + l * 4) = m[u];
    if (u != a) {
      const unsigned dst = L_RED + (a * 3 + ((u - a - 1) & 3)) * 4096 + l * 16;
#pragma unroll
      for (int g = 0; g < 4; g++) *(LAS f32x4*)(lds + dst + g * 1024) = f32x4{o[u][4 * g], o[u][4 * g + 1], o[u][4 * g + 2], o[u][4 * g + 3]};
    }
  }
  grp_arrive(lds + CA);
  f32x16 t = o[0]; float mo = m[0];
  if (a == 1) { t = o[1]; mo = m[1]; } else if (a == 2) { t = o[2]; mo = m[2]; } else if (a == 3) { t = o[3]; mo = m[3]; }
  cnt_wait(lds + CA, 4);
  float mw[4];
#pragma unroll
  for (int w = 0; w < 4; w++) mw[w] = *(LAS float*)(lds + L_M + (w * 4 + a) * 256 + l * 4);
  const float M = fmaxf(fmaxf(mw[0], mw[1]), fmaxf(mw[2], mw[3]));
  const float so = __builtin_amdgcn_exp2f(mo - M);
#pragma unroll
  for (int r = 0; r < 16; r++) t[r] *= so;
#pragma unroll
  for (int i = 1; i < 4; i++) {
    const int w = (a + i) & 3;
    const float sc = __builtin_amdgcn_exp2f(*(LAS float*)(lds + L_M + (w * 4 + a) * 256 + l * 4) - M);
#pragma unroll
    for (int g = 0; g < 4; g++) {
      f32x4 v = *(LAS f32x4*)(lds + L_RED + (w * 3 + (3 - i)) * 4096 + g * 1024 + l * 16);
#pragma unroll
      for (int e = 0; e < 4; e++) t[4 * g + e] = __builtin_fmaf(v[e], sc, t[4 * g + e]);
    }
  }
  const float lsum = half_sum(h ? t[12] : 0.f);
  const float sc = gate * __builtin_amdgcn_rcpf(lsum);
  float y[16];
#pragma unroll
  for (int r = 0; r < 16; r++) { y[r] = t[r] * sc; if (r >= 12 && h) y[r] = 0.f; }
  u32x4 f0 = {pk(y[0], y[1]), pk(y[2], y[3]), pk(y[4], y[5]), pk(y[6], y[7])};
  u32x4 f1 = {pk(y[8], y[9]), pk(y[10], y[11]), pk(y[12], y[13]), pk(y[14], y[15])};
  const unsigned oo = WS_O + ((2 * g64 + (a & 1)) * 32 + 2 * (2 * j + (a >> 1))) * 1024;
  st16_sc1(f0, sWS, l * 16, oo); st16_sc1(f1, sWS, l * 16, oo + 1024);
  wait_vm<0>();
  if (cnt_last(lds + CO, 4)) { if (l == 0) __builtin_amdgcn_raw_buffer_store_b32(tag, sFL, FL_O + (g64 * 8 + j) * 4, 0, AUX_SC1); }
  STAMP(ws, 0, 6);
}

__global__ void __launch_bounds__(512, 2) fused_kernel(const char* __restrict__ x, const char* __restrict__ wq, const char* __restrict__ wk,
                                                       const char* __restrict__ wv, const char* __restrict__ wo, const char* __restrict__ w1,
                                                       const char* __restrict__ b1, const char* __restrict__ w2, const char* __restrict__ b2,
                                                       const char* __restrict__ gw, char* __restrict__ ws, char* __restrict__ out) {
  extern __shared__ __attribute__((aligned(16))) char smem[];
  asm volatile("" :: "s"(x), "s"(wq), "s"(wk), "s"(wv), "s"(wo), "s"(w1), "s"(b1), "s"(w2), "s"(b2), "s"(gw), "s"(ws), "s"(out));
  ldsp lds = (ldsp)smem;
  const unsigned ldsb = (unsigned)(size_t)lds;
  const int b = blockIdx.x, xcd = b & 7, sl = b >> 3;
  const int g64 = 4 * xcd + (sl >> 3), j = sl & 7;
  const int w = __builtin_amdgcn_readfirstlane(threadIdx.x >> 6), l = threadIdx.x & 63;
  PROF_INIT
  STAMP(ws, 0, 0);
  const srd_t sWS = mk_srd(ws, (unsigned)WS_B), sFL = mk_srd(ws + WS_FL, 8192);
  unsigned epoch;
  asm volatile("s_nop 4\n\tbuffer_load_dword %0, off, %1, 0 sc1" : "=v"(epoch) : "s"(sFL) : "memory");
  unsigned gwv = 0;
  if (w >= 4) {
    const int f = w - 4;
    const srd_t sX = mk_srd(x, S * D * 2);
    const unsigned tok = 16 * f + (l >> 3);
    const unsigned vx0 = (64 * g64 + tok) * 896 + 16 * ((l & 7) ^ ((tok >> 1) & 7));
    const unsigned vx1 = (64 * g64 + tok + 8) * 896 + 16 * ((l & 7) ^ (((tok + 8) >> 1) & 7));
    asm volatile("s_nop 4");
#pragma unroll
    for (int ch = 0; ch < 7; ch++) {
      dma16(ldsb + L_X + (2 * f * 7 + ch) * 1024, vx0, sX, ch * 128);
      dma16(ldsb + L_X + ((2 * f + 1) * 7 + ch) * 1024, vx1, sX, ch * 128);
    }
    if (j < 4) {
      const srd_t sK = mk_srd(j < 2 ? wk : wv, D * DH * 2);
      asm volatile("s_nop 4");
#pragma unroll
      for (int i = 0; i < 6; i++) dma16(ldsb + L_WKV + (f + 4 * i) * 1024, l * 16, sK, (f + 4 * i) * 1024);
      if (f == 0) dma16(ldsb + L_WKV + 24 * 1024, l * 16, sK, 24 * 1024);
    }
    const int T0 = f < 3 ? 2 * (3 * j + f) : 48 + j;
    asm volatile("s_nop 4");
    dma4(ldsb + L_B1 + f * 256, l * 4, mk_srd(b1, F * 2), 64 * T0);
    if (f == 0) dma4(ldsb + L_B2, l * 4, mk_srd(b2, D * 2), 128 * j);
    STAMP(ws, 0, 14);
    bar();
    wait_vm<0>();
    grp_arrive(lds + CX);
    cnt_wait(lds + CX, 4);
  } else {
    const srd_t sG = mk_srd(gw, D * 2), sQ = mk_srd(wq, D * D * 2);
    asm volatile("s_nop 4\n\tbuffer_load_dword %0, %1, %2, 0 offen" : "=v"(gwv) : "v"((112 * w + 2 * l) * 2), "s"(sG) : "memory");
    const unsigned vq = (112 * w + (l >> 3)) * 896 + 112 * j + 16 * ((l & 7) ^ (((l >> 4) & 1) << 2));
    if (threadIdx.x < 16) *(LAS unsigned*)(lds + L_CNT + threadIdx.x * 4) = 0;
    lgkm0();
    bar();
#pragma unroll
    for (int i = 0; i < 14; i++) dma16(ldsb + L_WQ + (112 * w + 8 * i) * 128, vq, sQ, i * 8 * 896);
    if (w < 3) {
      const srd_t sW1 = mk_srd(w1, D * F * 2);
      const unsigned vW1 = (l >> 3) * 3584 + 128 * (3 * j + w) + 16 * ((l & 7) ^ (((l >> 4) & 1) << 2));
      asm volatile("s_nop 4");
#pragma unroll
      for (int i = 0; i < 6; i++) dma16(ldsb + L_R0 + w * 6144 + i * 1024, vW1, sW1, i * 8 * 3584);
    }
    STAMP(ws, 0, 8);
    wait_vm<14>();
  }
  asm volatile("" : "+v"(epoch));
  const unsigned tag = __builtin_amdgcn_readfirstlane(epoch) + 1u;
  STAMP(ws, 0, 1);
  Ptrs P = {x, wq, wk, wv, wo, w1, b1, w2, b2, gw, ws, out};
  if (w < 4) a_wave(P, lds, ldsb, g64, j, w, l, tag, gwv, sWS, sFL PROF_PASS);
  else { f_wave(P, lds, ldsb, g64, j, w - 4, l, tag, sWS, sFL PROF_PASS); }
  PROF_DUMP(ws);
}

extern "C" void kernel_launch(void* const* d_in, const int* in_sizes, int n_in, void* d_out, int out_size, void* d_ws, size_t ws_size,
                              hipStream_t stream) {
  (void)hipFuncSetAttribute((const void*)fused_kernel, hipFuncAttributeMaxDynamicSharedMemorySize, L_TOTAL);
  fused_kernel<<<256, 512, L_TOTAL, stream>>>((const char*)d_in[0], (const char*)d_in[1], (const char*)d_in[2], (const char*)d_in[3],
                                              (const char*)d_in[4], (const char*)d_in[6], (const char*)d_in[7], (const char*)d_in[8],
                                              (const char*)d_in[9], (const char*)d_in[10], (char*)d_ws, (char*)d_out);
}
```

```cpp
#include <hip/hip_runtime.h>
#include <hip/hip_bf16.h>
#include <stdint.h>

typedef __attribute__((ext_vector_type(8))) __bf16 bf16x8;
typedef __attribute__((ext_vector_type(4))) __bf16 bf16x4;
typedef __attribute__((ext_vector_type(2))) __bf16 bf16x2;
typedef __attribute__((ext_vector_type(16))) float f32x16;
typedef __attribute__((ext_vector_type(4))) float f32x4;
typedef __attribute__((ext_vector_type(2))) float f32x2;
typedef __attribute__((ext_vector_type(4))) short s16x4;
typedef __attribute__((ext_vector_type(8))) short s16x8;
typedef __attribute__((ext_vector_type(4))) unsigned u32x4;
typedef __attribute__((ext_vector_type(2))) unsigned u32x2;
#define LAS __attribute__((address_space(3)))
#define GAS __attribute__((address_space(1)))
typedef __amdgpu_buffer_rsrc_t srd_t;
typedef LAS char* ldsp;

constexpr int S = 2048, D = 448, NH = 16, DH = 28, F = 1792;
constexpr size_t WS_KV = 0;
constexpr size_t WS_Q  = WS_KV + 64 * 4 * 1024;
constexpr size_t WS_H  = WS_Q + 16 * 64 * 2 * 1024;
constexpr size_t WS_O  = WS_H + 64 * 112 * 1024;
constexpr size_t WS_G  = WS_O + 64 * 32 * 1024;
constexpr size_t WS_QN = WS_G + 2048 * 4;
constexpr size_t WS_KN = WS_QN + 16 * 64 * 4;
constexpr size_t WS_VM = WS_KN + 64 * 4;
constexpr size_t WS_B  = WS_VM + 64 * 4;

#define SRD_W3 0x00020000
__device__ __forceinline__ srd_t mk_srd(const void* p, unsigned bytes) {
  return __builtin_amdgcn_make_buffer_rsrc((void*)p, (short)0, (int)bytes, SRD_W3);
}
__device__ __forceinline__ void dma16(unsigned m0v, unsigned voff, srd_t srd, unsigned soff) {
  asm volatile("s_mov_b32 m0, %0\n\ts_nop 0\n\tbuffer_load_dwordx4 %1, %2, %3 offen lds"
               :: "s"(m0v), "v"(voff), "s"(srd), "s"(soff) : "memory");
}
__device__ __forceinline__ void dma4(unsigned m0v, unsigned voff, srd_t srd, unsigned soff) {
  asm volatile("s_mov_b32 m0, %0\n\ts_nop 0\n\tbuffer_load_dword %1, %2, %3 offen lds"
               :: "s"(m0v), "v"(voff), "s"(srd), "s"(soff) : "memory");
}
template <int N> __device__ __forceinline__ void wait_vm() { asm volatile("s_waitcnt vmcnt(%0)" :: "n"(N) : "memory"); }
__device__ __forceinline__ void bar() { asm volatile("s_barrier" ::: "memory"); }
__device__ __forceinline__ bf16x8 rd128(ldsp p) { return *(LAS bf16x8*)p; }
__device__ __forceinline__ bf16x8 rdtr(ldsp p0, ldsp p1) {
  s16x4 a = __builtin_amdgcn_ds_read_tr16_b64_v4i16((LAS s16x4*)p0);
  s16x4 b = __builtin_amdgcn_ds_read_tr16_b64_v4i16((LAS s16x4*)p1);
  s16x8 r = __builtin_shufflevector(a, b, 0, 1, 2, 3, 4, 5, 6, 7);
  return __builtin_bit_cast(bf16x8, r);
}
__device__ __forceinline__ f32x16 mfma(bf16x8 a, bf16x8 b, f32x16 c) {
  return __builtin_amdgcn_mfma_f32_32x32x16_bf16(a, b, c, 0, 0, 0);
}
__device__ __forceinline__ unsigned pk(float lo, float hi) {
  f32x2 v = {lo, hi};
  bf16x2 r = __builtin_convertvector(v, bf16x2);
  return __builtin_bit_cast(unsigned, r);
}
__device__ __forceinline__ float bflo(unsigned u) { return __uint_as_float(u << 16); }
__device__ __forceinline__ float bfhi(unsigned u) { return __uint_as_float(u & 0xffff0000u); }
template <int CTRL> __device__ __forceinline__ float dpp_f(float v) {
  return __int_as_float(__builtin_amdgcn_update_dpp(0, __float_as_int(v), CTRL, 0xf, 0xf, true));
}
__device__ __forceinline__ float wave_max_nn(float v) {
  v = fmaxf(v, dpp_f<0xB1>(v));
  v = fmaxf(v, dpp_f<0x4E>(v));
  v = fmaxf(v, dpp_f<0x141>(v));
  v = fmaxf(v, dpp_f<0x140>(v));
  unsigned a = __builtin_amdgcn_readlane(__float_as_uint(v), 0), b = __builtin_amdgcn_readlane(__float_as_uint(v), 16);
  unsigned c = __builtin_amdgcn_readlane(__float_as_uint(v), 32), d = __builtin_amdgcn_readlane(__float_as_uint(v), 48);
  a = a > b ? a : b; c = c > d ? c : d; a = a > c ? a : c;
  return __uint_as_float(a);
}
__device__ __forceinline__ float half_sum(float s) {
  auto r = __builtin_amdgcn_permlane32_swap(__float_as_uint(s), __float_as_uint(s), false, false);
  return __uint_as_float(r[0]) + __uint_as_float(r[1]);
}


#define GSB() __builtin_amdgcn_sched_barrier(0)
__device__ __forceinline__ u32x4 gelu8(const f32x16& a, int r0, f32x2 c1, f32x2 c2) {
  f32x2 x[4], p[4], e[4];
#pragma unroll
  for (int i = 0; i < 4; i++) { x[i] = f32x2{a[r0 + 2 * i], a[r0 + 2 * i + 1]}; p[i] = x[i] * x[i]; }
#pragma unroll
  for (int i = 0; i < 4; i++) p[i] = __builtin_elementwise_fma(p[i], c2, c1);
#pragma unroll
  for (int i = 0; i < 4; i++) p[i] = p[i] * x[i];
  GSB();
#pragma unroll
  for (int i = 0; i < 4; i++) { e[i][0] = __builtin_amdgcn_exp2f(p[i][0]); e[i][1] = __builtin_amdgcn_exp2f(p[i][1]); }
  GSB();
#pragma unroll
  for (int i = 0; i < 4; i++) e[i] = e[i] + f32x2{1.0f, 1.0f};
  GSB();
#pragma unroll
  for (int i = 0; i < 4; i++) { e[i][0] = __builtin_amdgcn_rcpf(e[i][0]); e[i][1] = __builtin_amdgcn_rcpf(e[i][1]); }
  GSB();
  u32x4 r;
#pragma unroll
  for (int i = 0; i < 4; i++) { x[i] = x[i] * e[i]; r[i] = pk(x[i][0], x[i][1]); }
  GSB();
  return r;
}


#define SB() __builtin_amdgcn_sched_barrier(0)
#define PIN(x) asm volatile("" : "+v"(x))
__device__ __forceinline__ void ld16_sc1(u32x4& d, unsigned voff, srd_t srd, unsigned soff) {
  asm volatile("buffer_load_dwordx4 %0, %1, %2, %3 offen sc1" : "=v"(d) : "v"(voff), "s"(srd), "s"(soff) : "memory");
}
template <int OFF> __device__ __forceinline__ void ld16o_sc1(u32x4& d, unsigned voff, srd_t srd, unsigned soff) {
  asm volatile("buffer_load_dwordx4 %0, %1, %2, %3 offen offset:%4 sc1" : "=v"(d) : "v"(voff), "s"(srd), "s"(soff), "n"(OFF) : "memory");
}
struct KVT { u32x4 k0, k1, v0, v1; };
__device__ __forceinline__ void kv_issue(KVT& t, unsigned voff, srd_t srd, unsigned soff) {
  ld16o_sc1<0>(t.k0, voff, srd, soff); ld16o_sc1<1024>(t.k1, voff, srd, soff);
  ld16o_sc1<2048>(t.v0, voff, srd, soff); ld16o_sc1<3072>(t.v1, voff, srd, soff);
}
template <int N> __device__ __forceinline__ void kv_wait(KVT& t) {
  asm volatile("s_waitcnt vmcnt(%4)" : "+v"(t.k0), "+v"(t.k1), "+v"(t.v0), "+v"(t.v1) : "n"(N) : "memory");
}
__device__ __forceinline__ bf16x8 asb(u32x4 v) { return __builtin_bit_cast(bf16x8, v); }
__device__ __forceinline__ f32x16 qk(const KVT& t, const bf16x8 (&q)[2]) {
  f32x16 z;
#pragma unroll
  for (int r = 0; r < 16; r++) z[r] = 0.f;
  z = mfma(asb(t.k0), q[0], z);
  return mfma(asb(t.k1), q[1], z);
}
struct Carry { float e[4]; unsigned p0, p1; };
#define EX(x) __builtin_amdgcn_exp2f(x)
template <bool has_prev, bool has_next>
__device__ __forceinline__ void attn_unit(f32x16& s, f32x16& o, f32x16& oprev, Carry& c, const u32x4& v0, const u32x4& v1prev,
                                          const u32x4& k0n, const u32x4& k1n, const bf16x8 (&qn)[2]) {
  f32x16 sn;
#pragma unroll
  for (int r = 0; r < 16; r++) sn[r] = 0.f;
  if (has_next) { sn = mfma(asb(k0n), qn[0], sn); PIN(sn); }
  SB();
  u32x4 pp;
  if (has_prev) { pp[0] = c.p0; pp[1] = c.p1; pp[2] = pk(c.e[0], c.e[1]); pp[3] = pk(c.e[2], c.e[3]); }
  float e0 = EX(s[0]), e1 = EX(s[1]), e2 = EX(s[2]), e3 = EX(s[3]);
  PIN(e0); PIN(e1); PIN(e2); PIN(e3);
  if (has_prev) PIN(pp);
  SB();
  if (has_prev) { oprev = mfma(asb(v1prev), asb(pp), oprev); PIN(oprev); }
  SB();
  float e4 = EX(s[4]), e5 = EX(s[5]), e6 = EX(s[6]), e7 = EX(s[7]);
  u32x4 p;
  p[0] = pk(e0, e1); p[1] = pk(e2, e3);
  PIN(e4); PIN(e5); PIN(e6); PIN(e7); PIN(p[0]); PIN(p[1]);
  SB();
  if (has_next) { sn = mfma(asb(k1n), qn[1], sn); PIN(sn); }
  SB();
  p[2] = pk(e4, e5); p[3] = pk(e6, e7);
  float e8 = EX(s[8]), e9 = EX(s[9]), e10 = EX(s[10]), e11 = EX(s[11]);
  PIN(p); PIN(e8); PIN(e9); PIN(e10); PIN(e11);
  SB();
  o = mfma(asb(v0), asb(p), o); PIN(o);
  SB();
  c.e[0] = EX(s[12]); c.e[1] = EX(s[13]); c.e[2] = EX(s[14]); c.e[3] = EX(s[15]);
  c.p0 = pk(e8, e9); c.p1 = pk(e10, e11);
  PIN(c.e[0]); PIN(c.e[1]); PIN(c.e[2]); PIN(c.e[3]); PIN(c.p0); PIN(c.p1);
  s = sn;
  SB();
}
template <int MODE>
__device__ __forceinline__ void attn_block(KVT (&kv)[4], f32x16& s, f32x16 (&o)[4], Carry& c, const bf16x8 (&q)[4][2], unsigned voff, srd_t srd,
                                           unsigned soff) {
#pragma unroll
  for (int t = 0; t < 4; t++) {
    KVT& cur = kv[t]; KVT& prv = kv[(t + 3) & 3]; KVT& nxt = kv[(t + 1) & 3];
    if (MODE == 1 && t == 0) attn_unit<false, true>(s, o[0], o[3], c, cur.v0, prv.v1, cur.k0, cur.k1, q[1]);
    else attn_unit<true, true>(s, o[0], o[3], c, cur.v0, prv.v1, cur.k0, cur.k1, q[1]);
    if (MODE != 2 || t == 0) kv_issue(prv, voff, srd, soff + (t + 3) * 4096);
    SB();
    attn_unit<true, true>(s, o[1], o[0], c, cur.v0, cur.v1, cur.k0, cur.k1, q[2]);
    attn_unit<true, true>(s, o[2], o[1], c, cur.v0, cur.v1, cur.k0, cur.k1, q[3]);
    if (MODE != 2 || t == 0) kv_wait<8>(nxt);
    else if (t == 1) kv_wait<4>(nxt);
    else if (t == 2) kv_wait<0>(nxt);
    SB();
    if (MODE == 2 && t == 3) attn_unit<true, false>(s, o[3], o[2], c, cur.v0, cur.v1, cur.k0, cur.k1, q[0]);
    else attn_unit<true, true>(s, o[3], o[2], c, cur.v0, cur.v1, nxt.k0, nxt.k1, q[0]);
  }
  if (MODE == 2) {
    u32x4 pp = {c.p0, c.p1, pk(c.e[0], c.e[1]), pk(c.e[2], c.e[3])};
    o[3] = mfma(asb(kv[3].v1), asb(pp), o[3]);
  }
}
__device__ __forceinline__ void attn_fast(f32x16 (&o)[4], const bf16x8 (&q)[4][2], KVT (&kv)[4], srd_t srd, unsigned soff0, int l) {
  const unsigned voff = l * 16;
  kv[3].v1 = kv[3].v0 = kv[3].k0 = kv[3].k1 = u32x4{0, 0, 0, 0};
  kv_wait<8>(kv[0]);
  f32x16 s = qk(kv[0], q[0]);
  Carry c;
  attn_block<1>(kv, s, o, c, q, voff, srd, soff0);
#pragma unroll 1
  for (int it = 1; it < 3; it++) {
    attn_block<0>(kv, s, o, c, q, voff, srd, soff0 + it * 16384);
  }
  attn_block<2>(kv, s, o, c, q, voff, srd, soff0 + 3 * 16384);
}
__device__ __forceinline__ void attn_safe(f32x16 (&o)[4], float (&m)[4], const bf16x8 (&q)[4][2], srd_t srd, unsigned soff0, int l) {
  const unsigned voff = l * 16;
#pragma unroll
  for (int u = 0; u < 4; u++) m[u] = -3.0e38f;
#pragma unroll 1
  for (int t = 0; t < 16; t++) {
    KVT kv;
    kv_issue(kv, voff, srd, soff0 + t * 4096);
    kv_wait<0>(kv);
#pragma unroll
    for (int u = 0; u < 4; u++) {
      f32x16 s = qk(kv, q[u]);
      float mx = s[0];
#pragma unroll
      for (int r = 1; r < 16; r++) mx = fmaxf(mx, s[r]);
      auto sw = __builtin_amdgcn_permlane32_swap(__float_as_uint(mx), __float_as_uint(mx), false, false);
      mx = fmaxf(__uint_as_float(sw[0]), __uint_as_float(sw[1]));
      const float mn = fmaxf(m[u], mx);
      const float al = __builtin_amdgcn_exp2f(m[u] - mn);
      m[u] = mn;
      u32x4 p0, p1;
#pragma unroll
      for (int i = 0; i < 4; i++) {
        p0[i] = pk(__builtin_amdgcn_exp2f(s[2 * i] - mn), __builtin_amdgcn_exp2f(s[2 * i + 1] - mn));
        p1[i] = pk(__builtin_amdgcn_exp2f(s[8 + 2 * i] - mn), __builtin_amdgcn_exp2f(s[8 + 2 * i + 1] - mn));
      }
#pragma unroll
      for (int r = 0; r < 16; r++) o[u][r] *= al;
      o[u] = mfma(asb(kv.v0), asb(p0), o[u]);
      o[u] = mfma(asb(kv.v1), asb(p1), o[u]);
    }
  }
}

__device__ __forceinline__ void grp_arrive(ldsp cnt) {
  asm volatile("s_waitcnt lgkmcnt(0)" ::: "memory");
  if ((threadIdx.x & 63) == 0) __hip_atomic_fetch_add((LAS unsigned*)cnt, 1u, __ATOMIC_RELAXED, __HIP_MEMORY_SCOPE_WORKGROUP);
}
__device__ __forceinline__ void grp_wait(ldsp cnt, unsigned target) {
  while (__builtin_amdgcn_readfirstlane(*(volatile LAS unsigned*)cnt) < target) __builtin_amdgcn_s_sleep(1);
  asm volatile("" ::: "memory");
}

constexpr size_t WS_FL = WS_B;
constexpr unsigned FL_KV = 4096, FL_H = 36864, FL_O = 69632, FL_HG = 102400, FL_SIZE = 106496;
constexpr int L_X = 0, L_WQ = 57344, L_WKV = L_WQ + 28672, L_RING = L_WKV + 25088, L_QX = L_RING + 3 * 14336, L_MISC = L_QX + 8192;
constexpr int L_B1 = L_MISC, L_B2 = L_B1 + 896, L_GATE = L_B2 + 128, L_QN = L_GATE + 256, L_CNT = L_QN + 16;
__device__ __forceinline__ unsigned ring_off(int f) { return f < 3 ? L_RING + f * 14336 : L_WKV; }
constexpr int L_TOTAL = 163840;
constexpr int C0 = L_CNT, C1 = L_CNT + 4, C2 = L_CNT + 8, C3 = L_CNT + 12, CH = L_CNT + 16, CA = L_CNT + 20, CO = L_CNT + 24, CF = L_CNT + 28, CX = L_CNT + 32;
constexpr int L_RED = L_X, L_M = L_X + 49152;
constexpr unsigned SPIN_LIMIT = 100000;
constexpr int AUX_SC1 = 16;
constexpr int FPRIO = 0;
constexpr int APRIO = 2;
constexpr int APRIO2 = 1;
constexpr int PPRIO = 3;
constexpr int EARLY = 0;
constexpr int WQ1PRIO = 2;
constexpr int HRING = 8;
constexpr int GPRIO = 2;
constexpr int F2PRIO = 0;

__device__ __forceinline__ void st16_sc1(u32x4 v, srd_t srd, unsigned voff, unsigned soff) {
  __builtin_amdgcn_raw_buffer_store_b128(v, srd, voff + soff, 0, AUX_SC1);
}
__device__ __forceinline__ void lgkm0() { asm volatile("s_waitcnt lgkmcnt(0)" ::: "memory"); }
__device__ __forceinline__ void cnt_wait(ldsp p, unsigned target) {
#pragma unroll 1
  for (unsigned it = 0; it < (1u << 22); it++) {
    if (__builtin_amdgcn_readfirstlane(*(volatile LAS unsigned*)p) >= target) break;
    __builtin_amdgcn_s_sleep(1);
  }
  asm volatile("" ::: "memory");
}
__device__ __forceinline__ void cnt_spin(ldsp p, unsigned target) {
#pragma unroll 1
  for (unsigned it = 0; it < (1u << 24); it++)
    if (__builtin_amdgcn_readfirstlane(*(volatile LAS unsigned*)p) >= target) break;
  asm volatile("" ::: "memory");
}
__device__ __forceinline__ bool cnt_last(ldsp p, unsigned n) {
  unsigned old = 0;
  if ((threadIdx.x & 63) == 0) old = __hip_atomic_fetch_add((LAS unsigned*)p, 1u, __ATOMIC_RELAXED, __HIP_MEMORY_SCOPE_WORKGROUP);
  return __builtin_amdgcn_readfirstlane(old) == n - 1;
}
__device__ __forceinline__ void poll4(srd_t srd, unsigned voff, unsigned tag) {
#pragma unroll 1
  for (unsigned it = 0; it < SPIN_LIMIT; it++) {
    unsigned v;
    asm volatile("buffer_load_dword %0, %1, %2, 0 offen sc1\n\ts_waitcnt vmcnt(0)" : "=v"(v) : "v"(voff), "s"(srd) : "memory");
    if (__builtin_amdgcn_ballot_w64(v != tag) == 0) break;
    __builtin_amdgcn_s_sleep(2);
  }
}
__device__ __forceinline__ u32x2 poll8(srd_t srd, unsigned voff, unsigned tag) {
  u32x2 v;
#pragma unroll 1
  for (unsigned it = 0; it < SPIN_LIMIT; it++) {
    asm volatile("buffer_load_dwordx2 %0, %1, %2, 0 offen sc1\n\ts_waitcnt vmcnt(0)" : "=v"(v) : "v"(voff), "s"(srd) : "memory");
    if (__builtin_amdgcn_ballot_w64(v[1] != tag) == 0) break;
    __builtin_amdgcn_s_sleep(2);
  }
  return v;
}

template <bool SWAP, int WSTEP, int WR2, int KS0, int NB, int SIG = -1>
__device__ __forceinline__ void proj_chain(f32x16& acc, ldsp lds, unsigned wb, unsigned xl) {
  bf16x8 w[2][7], x[2][7];
  ldsp wp = lds + wb;
  ldsp xp[4];
#pragma unroll
  for (int i = 0; i < 4; i++) xp[i] = lds + (xl ^ (32u * i));
#pragma unroll
  for (int i = 0; i < 7; i++) { w[0][i] = rdtr(wp + i * WSTEP, wp + i * WSTEP + WR2); x[0][i] = rd128(xp[(KS0 + i) & 3] + ((KS0 + i) >> 2) * 1024); }
  SB();
#pragma unroll
  for (int b = 0; b < NB; b++) {
#pragma unroll
    for (int i = 0; i < 7; i++) {
      const int ks = 7 * b + i;
      acc = SWAP ? mfma(x[b & 1][i], w[b & 1][i], acc) : mfma(w[b & 1][i], x[b & 1][i], acc);
      if (b < NB - 1) {
        const int kn = ks + 7;
        w[(b + 1) & 1][i] = rdtr(wp + kn * WSTEP, wp + kn * WSTEP + WR2); x[(b + 1) & 1][i] = rd128(xp[(KS0 + kn) & 3] + ((KS0 + kn) >> 2) * 1024);
      }
      SB();
    }
    if (SIG >= 0 && b == NB - 2) { grp_arrive(lds + SIG); SB(); }
  }
}

template <int I> struct IC { static constexpr int v = I; };
template <int I, int N, class Fn> __device__ __forceinline__ void static_for(Fn&& fn) {
  if constexpr (I < N) { fn(IC<I>{}); static_for<I + 1, N>(fn); }
}
template <int OFF> __device__ __forceinline__ void set_m0(unsigned base) {
  asm volatile("s_add_u32 m0, %0, %1" :: "s"(base), "n"(OFF) : "scc", "memory");
}
__device__ __forceinline__ void dma_go(unsigned voff, srd_t srd, unsigned soff) {
  asm volatile("buffer_load_dwordx4 %0, %1, %2 offen lds" :: "v"(voff), "s"(srd), "s"(soff) : "memory");
}

struct FCtx {
  ldsp lds; unsigned ldsb;
  unsigned ring;
  unsigned ringb;
  unsigned la0, la1;
  unsigned xl;
  unsigned vW1, vW1b, vW2, vW2b;
  srd_t sW1, sW2, sWO;
  unsigned w2o, woo;
  int f, j;
};

template <int NSUB> struct F1Frag { bf16x8 a[NSUB]; bf16x8 b[2]; };
template <int NSUB> __device__ __forceinline__ void f1_issue_w1(const FCtx& c, unsigned ldsdst, int ks) {
  dma16(c.ldsb + ldsdst, c.vW1, c.sW1, ks * (16 * 3584));
  if (NSUB == 2) dma16(c.ldsb + ldsdst + 1024, c.vW1, c.sW1, ks * (16 * 3584) + 8 * 3584);
}
template <int NSUB> __device__ __forceinline__ void f1_load(F1Frag<NSUB>& fr, ldsp a0, ldsp a1, ldsp xp) {
  constexpr int R2 = NSUB == 2 ? 512 : 256;
  fr.a[0] = rdtr(a0, a0 + R2);
  if (NSUB == 2) fr.a[NSUB - 1] = rdtr(a1, a1 + R2);
  fr.b[0] = rd128(xp); fr.b[1] = rd128(xp + 28672);
}
template <int NSUB> __device__ __forceinline__ void f1_mma(const F1Frag<NSUB>& fr, f32x16 (&acc)[2][2]) {
#pragma unroll
  for (int n = 0; n < NSUB; n++) { acc[n][0] = mfma(fr.a[n], fr.b[0], acc[n][0]); acc[n][1] = mfma(fr.a[n], fr.b[1], acc[n][1]); }
}
template <int NSUB, int E>
__device__ __forceinline__ void f1_step(const FCtx& c, F1Frag<NSUB>& cur, F1Frag<NSUB>& nxt, f32x16 (&acc)[2][2], ldsp pa0, ldsp pa1,
                                        const ldsp (&xp)[4], unsigned& soff) {
  constexpr int R2 = NSUB == 2 ? 512 : 256;
  constexpr int SL = (E % 7) * 2048, SN = ((E + 1) % 7) * 2048;
  constexpr bool W1 = E < 21;
  constexpr int XO = ((E + 1) >> 2) * 1024;
  wait_vm<NSUB == 2 ? 10 : 5>();
  if (E == 21) soff = c.w2o;
  SB();
  acc[0][0] = mfma(cur.a[0], cur.b[0], acc[0][0]);
  SB();
  if (E < 27) { nxt.a[0] = rdtr(pa0 + SN, pa0 + SN + R2); nxt.b[0] = rd128(xp[(E + 1) & 3] + XO); }
  SB();
  acc[0][1] = mfma(cur.a[0], cur.b[1], acc[0][1]);
  SB();
  if (E < 27) { nxt.b[1] = rd128(xp[(E + 1) & 3] + XO + 28672); if (NSUB == 2) nxt.a[NSUB - 1] = rdtr(pa1 + SN, pa1 + SN + R2); }
  set_m0<SL>(c.ringb);
  SB();
  if (NSUB == 2) { acc[1][0] = mfma(cur.a[NSUB - 1], cur.b[0], acc[1][0]); } else { asm volatile("s_waitcnt lgkmcnt(%0)" :: "n"(E < 27 ? 4 : 0) : "memory"); }
  SB();
  dma_go(W1 ? c.vW1 : c.vW2, W1 ? c.sW1 : c.sW2, soff);
  if (NSUB == 2 || !W1) set_m0<SL + 1024>(c.ringb);
  SB();
  if (NSUB == 2) { acc[1][1] = mfma(cur.a[NSUB - 1], cur.b[1], acc[1][1]); } else if (!W1) asm volatile("s_nop 0");
  SB();
  if (NSUB == 2 || !W1) dma_go(W1 ? c.vW1b : c.vW2b, W1 ? c.sW1 : c.sW2, soff);
  soff += W1 ? 16 * 3584 : 16 * 896;
  SB();
}
template <int NSUB> __device__ __forceinline__ void ffn1(FCtx& c, f32x16 (&acc)[2][2], char* ws) {
  F1Frag<NSUB> f0, f1;
  ldsp xp[4];
#pragma unroll
  for (int i = 0; i < 4; i++) xp[i] = c.lds + (c.xl ^ (32u * i));
  unsigned soff = 7 * (16 * 3584);
  ldsp pa0 = c.lds + c.la0, pa1 = c.lds + c.la1;
  wait_vm<NSUB == 2 ? 12 : 6>();
  f1_load<NSUB>(f0, pa0, pa1, xp[0]);
  static_for<0, 28>([&](auto ic) {
    constexpr int E = decltype(ic)::v;
    if constexpr ((E & 1) == 0) f1_step<NSUB, E>(c, f0, f1, acc, pa0, pa1, xp, soff);
    else f1_step<NSUB, E>(c, f1, f0, acc, pa0, pa1, xp, soff);
  });
}

struct AFr { bf16x8 a[2]; };
struct HB { u32x4 t0, t1; };
template <int N> __device__ __forceinline__ void hb_wait(HB& h) { asm volatile("s_waitcnt vmcnt(%2)" : "+v"(h.t0), "+v"(h.t1) : "n"(N) : "memory"); }
constexpr int HR = HRING;
constexpr int f2_nd(int s) { return s + 7 < 28 ? 2 : 0; }
constexpr int f2_nh(int s) { return s + HR < 28 ? 2 : 0; }
constexpr int f2_sum(int a, int b) { int r = 0; for (int s = a; s < b; s++) r += f2_nd(s) + f2_nh(s); return r; }
constexpr int f2_wait(int S) {
  int wh = S < HR ? 2 * (HR - 1 - S) + f2_sum(0, S) : f2_sum(S - HR + 1, S);
  if (S + 1 >= 7 && S + 1 < 28) { int wi = f2_nh(S + 1 - 7) + f2_sum(S + 1 - 6, S); wh = wi < wh ? wi : wh; }
  return wh;
}
template <int S>
__device__ __forceinline__ void f2_step(const FCtx& c, AFr& cur, AFr& nxt, HB& h, f32x16 (&acc)[2][2], ldsp pa0, ldsp pa1, unsigned& soff,
                                        unsigned hv, srd_t sH, unsigned& hs, unsigned hd) {
  constexpr int SL = (S % 7) * 2048, SN = ((S + 1) % 7) * 2048;
  constexpr bool W2 = S < 21;
  hb_wait<f2_wait(S)>(h);
  SB();
  acc[0][0] = mfma(cur.a[0], asb(h.t0), acc[0][0]);
  SB();
  if (S < 27) nxt.a[0] = rdtr(pa0 + SN, pa0 + SN + 1024);
  SB();
  acc[0][1] = mfma(cur.a[0], asb(h.t1), acc[0][1]);
  SB();
  if (S < 27) nxt.a[1] = rdtr(pa1 + SN, pa1 + SN + 1024);
  if (W2) set_m0<SL>(c.ringb);
  SB();
  acc[1][0] = mfma(cur.a[1], asb(h.t0), acc[1][0]);
  SB();
  if (W2) { dma_go(c.vW2, c.sW2, soff); set_m0<SL + 1024>(c.ringb); }
  SB();
  acc[1][1] = mfma(cur.a[1], asb(h.t1), acc[1][1]);
  SB();
  if (W2) { dma_go(c.vW2b, c.sW2, soff); soff += 16 * 896; }
  if (S + HR < 28) { ld16o_sc1<0>(h.t0, hv, sH, hs); ld16o_sc1<0>(h.t1, hv, sH, hs + hd); hs += 1024; }
  SB();
}
constexpr unsigned SENT = 0xffffffffu;
__device__ __forceinline__ unsigned umax3(unsigned a, unsigned b, unsigned c) { a = a > b ? a : b; return a > c ? a : c; }
__device__ __forceinline__ bool hb_pending(const HB& h) {
  const unsigned m = umax3(umax3(umax3(h.t0[0], h.t0[1], h.t0[2]), h.t0[3], h.t1[0]), umax3(h.t1[1], h.t1[2], h.t1[3]), 0u);
  return __builtin_amdgcn_ballot_w64(m == SENT) != 0;
}
__device__ __forceinline__ void ffn2_tail(const FCtx& c, f32x16 (&acc)[2][2], int g64, int l, srd_t sWS, float& og0, float& og1, char* ws) {
  ldsp pa0 = c.lds + c.la0, pa1 = c.lds + c.la1;
  const unsigned hv = l * 16;
  const int ft = c.f & 1;
  unsigned hs = WS_H + ((2 * g64 + ft) * 112 + 28 * c.f) * 1024;
  const unsigned hd = ft ? 0u - 112 * 1024 : 112 * 1024;
  HB hb[HR];
#pragma unroll
  for (int i = 0; i < HR; i++) { ld16_sc1(hb[i].t0, hv, sWS, hs + i * 1024); ld16_sc1(hb[i].t1, hv, sWS, hs + i * 1024 + hd); }
  hs += HR * 1024;
#pragma unroll
  for (int n = 0; n < 2; n++)
#pragma unroll
    for (int t = 0; t < 2; t++)
#pragma unroll
      for (int r = 0; r < 16; r++) acc[n][t][r] = 0.f;
  AFr a0, a1;
  a0.a[0] = rdtr(pa0, pa0 + 1024); a0.a[1] = rdtr(pa1, pa1 + 1024);
  unsigned soff = c.w2o + 7 * (16 * 896);
  static_for<0, 28>([&](auto ic) {
    constexpr int S = decltype(ic)::v;
    if constexpr ((S & 1) == 0) f2_step<S>(c, a0, a1, hb[S % HR], acc, pa0, pa1, soff, hv, sWS, hs, hd);
    else f2_step<S>(c, a1, a0, hb[S % HR], acc, pa0, pa1, soff, hv, sWS, hs, hd);
  });
  const unsigned x7 = L_WQ + c.f * 2048;
  lgkm0();
#pragma unroll
  for (int t = 0; t < 8; t++) {
    const unsigned dst = t < 7 ? c.ringb + t * 2048 : c.ldsb + x7, so = c.woo + (28 * (t >> 1) + 16 * (t & 1)) * 896;
    dma16(dst, c.vW2, c.sWO, so); dma16(dst + 1024, c.vW2b, c.sWO, so);
  }
  cnt_wait(c.lds + C2, 4);
  og0 = 1.0f - *(LAS float*)(c.lds + L_GATE + 128 * ft + (l & 31) * 4); og1 = 1.0f - *(LAS float*)(c.lds + L_GATE + 128 * (1 - ft) + (l & 31) * 4);
#pragma unroll
  for (int n = 0; n < 2; n++)
#pragma unroll
    for (int r = 0; r < 16; r++) { acc[n][0][r] *= og0; acc[n][1][r] *= og1; }
  const unsigned os = WS_O + ((2 * g64 + ft) * 32 + 8 * c.f) * 1024, od = ft ? 0u - 32768 : 32768;
  const unsigned pv = WS_O + ((2 * g64) * 32 + 8 * c.f) * 1024 + (l & 7) * 1024 + ((l >> 3) & 1) * 32768 + 1020;
#pragma unroll 1
  for (unsigned it = 0; it < SPIN_LIMIT; it++) {
    unsigned v;
    asm volatile("buffer_load_dword %0, %1, %2, 0 offen sc1\n\ts_waitcnt vmcnt(0)" : "=v"(v) : "v"(pv), "s"(sWS) : "memory");
    if (__builtin_amdgcn_ballot_w64(v == SENT) == 0) break;
    __builtin_amdgcn_s_sleep(1);
  }
  HB ob[8];
#pragma unroll
  for (int t = 0; t < 8; t++) { ld16_sc1(ob[t].t0, hv, sWS, os + t * 1024); ld16_sc1(ob[t].t1, hv, sWS, os + t * 1024 + od); }
  const unsigned d7 = x7 - c.ring;
  a0.a[0] = rdtr(pa0, pa0 + 1024); a0.a[1] = rdtr(pa1, pa1 + 1024);
  static_for<0, 8>([&](auto ic) {
    constexpr int T = decltype(ic)::v;
    constexpr int SN = (T + 1) * 2048;
    AFr& cur = (T & 1) ? a1 : a0; AFr& nxt = (T & 1) ? a0 : a1;
    if (T < 6) { nxt.a[0] = rdtr(pa0 + SN, pa0 + SN + 1024); nxt.a[1] = rdtr(pa1 + SN, pa1 + SN + 1024); }
    if (T == 6) { nxt.a[0] = rdtr(pa0 + d7, pa0 + d7 + 1024); nxt.a[1] = rdtr(pa1 + d7, pa1 + d7 + 1024); }
    hb_wait<14 - 2 * T>(ob[T]);
    if (hb_pending(ob[T])) {
#pragma unroll 1
      for (unsigned it = 0; it < SPIN_LIMIT; it++) {
        ld16_sc1(ob[T].t0, hv, sWS, os + T * 1024); ld16_sc1(ob[T].t1, hv, sWS, os + T * 1024 + od);
        hb_wait<0>(ob[T]);
        if (!hb_pending(ob[T])) break;
      }
    }
    acc[0][0] = mfma(cur.a[0], asb(ob[T].t0), acc[0][0]); acc[0][1] = mfma(cur.a[0], asb(ob[T].t1), acc[0][1]);
    acc[1][0] = mfma(cur.a[1], asb(ob[T].t0), acc[1][0]); acc[1][1] = mfma(cur.a[1], asb(ob[T].t1), acc[1][1]);
    SB();
  });
}

__device__ __forceinline__ void h_arrive(ldsp lds, srd_t sFL, int g64, int j, int l, unsigned tag) {
  if (cnt_last(lds + CH, 8)) { if (l < 8) __builtin_amdgcn_raw_buffer_store_b32(tag, sFL, l < 7 ? FL_H + (g64 * 8 + l) * 128 + j * 4 : FL_HG + (g64 * 8 + j) * 4, 0, AUX_SC1); }
}
__device__ __forceinline__ void add3(f32x16& t, ldsp p0, ldsp p1, ldsp p2) {
  f32x4 v[12];
#pragma unroll
  for (int g = 0; g < 4; g++) { v[g] = *(LAS f32x4*)(p0 + g * 1024); v[4 + g] = *(LAS f32x4*)(p1 + g * 1024); v[8 + g] = *(LAS f32x4*)(p2 + g * 1024); }
  SB();
#pragma unroll
  for (int i = 0; i < 12; i++) {
    const int g = i & 3;
    const f32x2 lo = f32x2{t[4 * g], t[4 * g + 1]} + f32x2{v[i][0], v[i][1]}, hi = f32x2{t[4 * g + 2], t[4 * g + 3]} + f32x2{v[i][2], v[i][3]};
    t[4 * g] = lo[0]; t[4 * g + 1] = lo[1]; t[4 * g + 2] = hi[0]; t[4 * g + 3] = hi[1];
  }
}
struct Ptrs { const char *x, *wq, *wk, *wv, *wo, *w1, *b1, *w2, *b2, *gw; char *ws, *out; };

__device__ __forceinline__ void zero16(f32x16& a) {
#pragma unroll
  for (int r = 0; r < 16; r++) a[r] = 0.f;
}

__device__ __forceinline__ void f_wave(const Ptrs& P, ldsp lds, unsigned ldsb, int g64, int j, int f, int l, unsigned epoch, srd_t sWS, srd_t sFL) {
  const int h = l >> 5;
  FCtx c;
  c.lds = lds; c.ldsb = ldsb; c.f = f; c.j = j;
  c.ring = ring_off(f); c.ringb = ldsb + c.ring;
  c.w2o = (unsigned)(28 * f) * (16 * 896); c.woo = (unsigned)(28 * 4 * f) * 896;
  c.sW1 = mk_srd(P.w1, D * F * 2); c.sW2 = mk_srd(P.w2, D * F * 2); c.sWO = mk_srd(P.wo, D * D * 2);
  const unsigned q4 = (l & 15) >> 2, g1 = (l >> 4) & 1;
  const unsigned swz = 16 * ((l & 7) ^ (((l >> 4) & 1) << 2));
  c.vW2 = (l >> 3) * 896 + 128 * j + swz; c.vW2b = c.vW2 + 8 * 896;
  const unsigned cpart = 16 * ((2 * g1 + ((l & 3) >> 1)) ^ (((q4 >> 1) & 1) << 2)) + 8 * (l & 1);
  c.xl = L_X + ((l & 31) >> 3) * 7168 + (l & 7) * 128 + 16 * (h ^ (((l & 31) >> 1) & 7));
  char* ws = P.ws;
  f32x16 acc[2][2];
  const int nsub = f < 3 ? 2 : 1;
  const int T0 = f < 3 ? 2 * (3 * j + f) : 48 + j;
  asm volatile("s_nop 4");
  if (f < 3) {
    c.vW1 = (l >> 3) * 3584 + 64 * T0 + swz; c.vW1b = c.vW1 + 8 * 3584;
    c.la0 = (8 * h + q4) * 128 + cpart; c.la1 = (c.la0 ^ 64) + c.ring; c.la0 += c.ring;
    __builtin_amdgcn_s_setprio(FPRIO);
#pragma unroll
    for (int i = 0; i < EARLY; i++) f1_issue_w1<2>(c, c.ring + i * 2048, i);
    wait_vm<2 * EARLY>();
  } else {
    c.vW1 = (l >> 2) * 3584 + 64 * T0 + 16 * (l & 3); c.vW1b = c.vW1;
    c.la0 = (8 * h + q4) * 64 + 32 * g1 + 8 * (l & 3) + c.ring; c.la1 = c.la0;
    wait_vm<0>();
  }
  asm volatile("" : "+v"(epoch));
  const unsigned tag = __builtin_amdgcn_readfirstlane(epoch) + 1u;
  __builtin_amdgcn_s_setprio(FPRIO);
  grp_arrive(lds + CX);
  if (f < 3) {
#pragma unroll
    for (int i = EARLY; i < 7; i++) f1_issue_w1<2>(c, c.ring + i * 2048, i);
  }
  cnt_wait(lds + CX, 4);
  if (f == 3) {
    if (j < 4) {
      __builtin_amdgcn_s_setprio(3);
      const int ts = j & 1, t32 = 2 * g64 + ts;
      const unsigned wb = L_WKV + (8 * h + q4) * 56 + 32 * g1 + 8 * (l & 3);
      const unsigned xo = ts * 28672;
      f32x16 a; zero16(a);
      if (j < 2) {
        proj_chain<false, 896, 224, 0, 4>(a, lds, wb, c.xl + xo);
        float k[16]; float ss = 0.f;
#pragma unroll
        for (int r = 0; r < 16; r++) { k[r] = a[r]; if (r >= 12 && h) k[r] = 0.f; ss = __builtin_fmaf(k[r], k[r], ss); }
        u32x4 f0 = {pk(k[0], k[1]), pk(k[2], k[3]), pk(k[4], k[5]), pk(k[6], k[7])};
        u32x4 f1 = {pk(k[8], k[9]), pk(k[10], k[11]), pk(k[12], k[13]), pk(k[14], k[15])};
        st16_sc1(f0, sWS, WS_KV + t32 * 4096 + l * 16, 0); st16_sc1(f1, sWS, WS_KV + t32 * 4096 + 1024 + l * 16, 0);
        const float mx = wave_max_nn(half_sum(ss));
        wait_vm<0>();
        if (l < 32) __builtin_amdgcn_raw_buffer_store_b64(u32x2{__float_as_uint(mx), tag}, sFL, FL_KV + l * 1024 + t32 * 16, 0, AUX_SC1);
      } else {
        proj_chain<true, 896, 224, 0, 4>(a, lds, wb, c.xl + xo);
        float v[16]; float mx = 0.f;
        const int d = l & 31;
#pragma unroll
        for (int r = 0; r < 16; r++) { v[r] = d < 28 ? a[r] : 0.f; mx = fmaxf(mx, fabsf(v[r])); if (d == 28) v[r] = 1.0f; }
        u32x4 f0 = {pk(v[0], v[1]), pk(v[2], v[3]), pk(v[4], v[5]), pk(v[6], v[7])};
        u32x4 f1 = {pk(v[8], v[9]), pk(v[10], v[11]), pk(v[12], v[13]), pk(v[14], v[15])};
        st16_sc1(f0, sWS, WS_KV + t32 * 4096 + 2048 + l * 16, 0); st16_sc1(f1, sWS, WS_KV + t32 * 4096 + 3072 + l * 16, 0);
        mx = wave_max_nn(mx);
        wait_vm<0>();
        if (l < 32) __builtin_amdgcn_raw_buffer_store_b64(u32x2{__float_as_uint(mx), tag}, sFL, FL_KV + l * 1024 + t32 * 16 + 8, 0, AUX_SC1);
      }
      __builtin_amdgcn_s_setprio(FPRIO);
    }
    lgkm0();
#pragma unroll
    for (int i = 0; i < 7; i++) f1_issue_w1<1>(c, c.ring + i * 2048, i);
  }
#pragma unroll
  for (int n = 0; n < 2; n++)
#pragma unroll
    for (int m = 0; m < 4; m++) {
      u32x2 v = *(LAS u32x2*)(lds + L_B1 + f * 256 + (32 * n + 8 * m + 4 * h) * 2);
#pragma unroll
      for (int t = 0; t < 2; t++) {
        acc[n][t][4 * m] = bflo(v[0]); acc[n][t][4 * m + 1] = bfhi(v[0]); acc[n][t][4 * m + 2] = bflo(v[1]); acc[n][t][4 * m + 3] = bfhi(v[1]);
      }
    }
  if (f < 3) ffn1<2>(c, acc, ws); else ffn1<1>(c, acc, ws);
  lgkm0();
  grp_arrive(lds + C3);
  __builtin_amdgcn_s_setprio(GPRIO);
  {
    f32x2 c1 = {-2.3022082f, -2.3022082f}, c2 = {-2.3022082f * 0.044715f, -2.3022082f * 0.044715f};
    asm volatile("" : "+v"(c1), "+v"(c2));
#pragma unroll
    for (int n = 0; n < 2; n++) {
      if (n < nsub) {
#pragma unroll
        for (int t = 0; t < 2; t++) {
          const u32x4 f0 = gelu8(acc[n][t], 0, c1, c2), f1 = gelu8(acc[n][t], 8, c1, c2);
          const unsigned so = WS_H + ((2 * g64 + t) * 112 + 2 * (T0 + n)) * 1024;
          st16_sc1(f0, sWS, l * 16, so); st16_sc1(f1, sWS, l * 16, so + 1024);
        }
      }
    }
  }
  wait_vm<0>();
  h_arrive(lds, sFL, g64, j, l, tag);
  if (j == 7) {
    if (g64 == 0 && f == 0) {
      poll4(sFL, FL_HG + l * 4, tag); poll4(sFL, FL_HG + 256 + l * 4, tag); poll4(sFL, FL_HG + 512 + l * 4, tag); poll4(sFL, FL_HG + 768 + l * 4, tag);
      if (l == 0) __builtin_amdgcn_raw_buffer_store_b32(tag, sFL, 0, 0, AUX_SC1);
    }
    return;
  }
  c.la0 = ((4 * h + q4) * 128 + cpart) ^ (64 * (f >> 1)); c.la1 = (c.la0 ^ 64) + c.ring; c.la0 += c.ring;
  __builtin_amdgcn_s_setprio(F2PRIO);
  poll4(sFL, FL_H + (g64 * 8 + j) * 128 + (l & 7) * 4, tag);
  float og0, og1;
  ffn2_tail(c, acc, g64, l, sWS, og0, og1, ws);
  lgkm0();
#pragma unroll
  for (int qd = 1; qd < 4; qd++) {
    const f32x16& a = acc[qd >> 1][qd & 1];
#pragma unroll
    for (int g = 0; g < 4; g++)
      *(LAS f32x4*)(lds + c.ring + (qd - 1) * 4096 + g * 1024 + l * 16) = f32x4{a[4 * g], a[4 * g + 1], a[4 * g + 2], a[4 * g + 3]};
  }
  grp_arrive(lds + CF);
  f32x16 t = acc[0][0];
#pragma unroll
  for (int m = 0; m < 4; m++) {
    u32x2 v = *(LAS u32x2*)(lds + L_B2 + (32 * (f >> 1) + 8 * m + 4 * h) * 2);
    t[4 * m] = __builtin_fmaf(og0, bflo(v[0]), t[4 * m]); t[4 * m + 1] = __builtin_fmaf(og0, bfhi(v[0]), t[4 * m + 1]);
    t[4 * m + 2] = __builtin_fmaf(og0, bflo(v[1]), t[4 * m + 2]); t[4 * m + 3] = __builtin_fmaf(og0, bfhi(v[1]), t[4 * m + 3]);
  }
  cnt_spin(lds + CF, 4);
  add3(t, lds + ring_off(f ^ 1) + l * 16, lds + ring_off(f ^ 2) + 4096 + l * 16, lds + ring_off(f ^ 3) + 8192 + l * 16);
  {
    unsigned p[8];
#pragma unroll
    for (int i = 0; i < 8; i++) p[i] = pk(t[2 * i], t[2 * i + 1]);
    const srd_t sOut = mk_srd(P.out, S * D * 2);
    const unsigned row = (64 * g64 + 32 * (f & 1) + (l & 31)) * 896 + 128 * j + 64 * (f >> 1) + 16 * h;
#pragma unroll
    for (int m = 0; m < 2; m++) {
      auto s0 = __builtin_amdgcn_permlane32_swap(p[4 * m], p[4 * m + 2], false, false);
      auto s1 = __builtin_amdgcn_permlane32_swap(p[4 * m + 1], p[4 * m + 3], false, false);
      u32x4 v = {s0[0], s1[0], s0[1], s1[1]};
      __builtin_amdgcn_raw_buffer_store_b128(v, sOut, row + 32 * m, 0, 0);
    }
  }
}

__device__ __forceinline__ void a_wave(const Ptrs& P, ldsp lds, unsigned ldsb, int g64, int j, int a, int l, unsigned tag, unsigned gwv,
                                       u32x4 (&wq1)[7], srd_t sWS, srd_t sFL) {
  const int h = l >> 5;
  char* ws = P.ws;
  const unsigned gad = L_WQ + (56 * a + 2 * (l & 31)) * 128 + 16 * (7 ^ ((l & 1) << 2));
  bf16x8 xf[28];
  {
    const unsigned xl = L_X + (a & 1) * 28672 + ((l & 31) >> 3) * 7168 + (l & 7) * 128 + 16 * (h ^ (((l & 31) >> 1) & 7));
    cnt_wait(lds + CX, 4);
#pragma unroll
    for (int i = 0; i < 28; i++) xf[i] = rd128(lds + (xl ^ (32u * (i & 3))) + (i >> 2) * 1024);
#pragma unroll
    for (int i = 0; i < 28; i++) PIN(xf[i]);
  }
  wait_vm<7>();
  asm volatile("" : "+v"(gwv));
  if (l < 28) {
    *(LAS unsigned short*)(lds + gad) = (unsigned short)gwv;
    *(LAS unsigned short*)(lds + gad + 128) = (unsigned short)(gwv >> 16);
  }
  grp_arrive(lds + C0);
  const int hb = a >> 1, ts = a & 1;
  const unsigned q4 = (l & 15) >> 2, g1 = (l >> 4) & 1;
  const unsigned u8 = 7 * hb + 4 * g1 + (l & 3);
  const unsigned wb = L_WQ + (8 * h + q4) * 128 + 16 * ((u8 >> 1) ^ (((q4 >> 1) & 1) << 2)) + 8 * (u8 & 1);
  cnt_wait(lds + C0, 4);
  f32x16 acc; zero16(acc);
  bf16x8 wf[14];
#pragma unroll
  for (int i = 0; i < 14; i++) wf[i] = rdtr(lds + wb + i * 2048, lds + wb + i * 2048 + 512);
  grp_arrive(lds + C1);
  SB();
#pragma unroll
  for (int i = 0; i < 14; i++) acc = mfma(wf[i], xf[i], acc);
  PIN(acc);
  SB();
  asm volatile("s_waitcnt vmcnt(0)" : "+v"(wq1[0]), "+v"(wq1[1]), "+v"(wq1[2]), "+v"(wq1[3]), "+v"(wq1[4]), "+v"(wq1[5]), "+v"(wq1[6]) :: "memory");
  cnt_spin(lds + C1, 4);
#pragma unroll
  for (int i = 0; i < 7; i++) *(LAS u32x4*)(lds + L_WQ + (56 * a + 8 * i) * 128 + l * 16) = wq1[i];
  if (l >= 32 && l < 60) {
    *(LAS unsigned short*)(lds + gad) = (unsigned short)gwv;
    *(LAS unsigned short*)(lds + gad + 128) = (unsigned short)(gwv >> 16);
  }
  grp_arrive(lds + C1);
  h_arrive(lds, sFL, g64, j, l, tag);
  const unsigned kvf = FL_KV + g64 * 1024 + (32 * a + (l & 31)) * 8;
  cnt_spin(lds + C1, 8);
#pragma unroll
  for (int i = 0; i < 14; i++) wf[i] = rdtr(lds + wb + i * 2048, lds + wb + i * 2048 + 512);
  SB();
#pragma unroll
  for (int i = 0; i < 14; i++) acc = mfma(wf[i], xf[14 + i], acc);
  PIN(acc);
  SB();
  u32x2 fl;
  asm volatile("buffer_load_dwordx2 %0, %1, %2, 0 offen sc1" : "=v"(fl) : "v"(kvf), "s"(sFL) : "memory");
  {
    const float qs = 0.18898223650461362f * 1.4426950408889634f;
    const float gt = __builtin_amdgcn_rcpf(1.0f + __builtin_amdgcn_exp2f(acc[12] * -1.4426950408889634f));
    float q[16]; float ss = 0.f;
#pragma unroll
    for (int r = 0; r < 16; r++) { q[r] = acc[r] * qs; if (r >= 12 && h) q[r] = 0.f; ss = __builtin_fmaf(q[r], q[r], ss); }
    u32x4 f0 = {pk(q[0], q[1]), pk(q[2], q[3]), pk(q[4], q[5]), pk(q[6], q[7])};
    u32x4 f1 = {pk(q[8], q[9]), pk(q[10], q[11]), pk(q[12], q[13]), pk(q[14], q[15])};
    const float mx = wave_max_nn(half_sum(ss));
    *(LAS u32x4*)(lds + L_QX + a * 2048 + l * 16) = f0; *(LAS u32x4*)(lds + L_QX + a * 2048 + 1024 + l * 16) = f1;
    if (l == 0) *(LAS float*)(lds + L_QN + a * 4) = mx;
    if (hb && h) *(LAS float*)(lds + L_GATE + (32 * ts + (l & 31)) * 4) = gt;
  }
  grp_arrive(lds + C2);
  asm volatile("s_waitcnt vmcnt(0)" : "+v"(fl) :: "memory");
  if (__builtin_amdgcn_ballot_w64(fl[1] != tag) != 0) fl = poll8(sFL, kvf, tag);
  const srd_t sKV = mk_srd(ws + WS_KV, 64 * 4096);
  KVT kv[4];
  asm volatile("s_nop 4");
  kv_issue(kv[0], l * 16, sKV, a * 16 * 4096);
  const float st = __uint_as_float(fl[0]);
  const float kn = wave_max_nn((l & 1) ? 0.f : st), vm = wave_max_nn((l & 1) ? st : 0.f);
  cnt_wait(lds + C2, 4);
  bf16x8 q[4][2];
#pragma unroll
  for (int u = 0; u < 4; u++) {
    const unsigned qa = L_QX + ((a + u) & 3) * 2048 + l * 16;
    q[u][0] = rd128(lds + qa); q[u][1] = rd128(lds + qa + 1024);
  }
  const f32x4 qn4 = *(LAS f32x4*)(lds + L_QN);
  const float gate = *(LAS float*)(lds + L_GATE + (32 * (a & 1) + (l & 31)) * 4);
  kv_issue(kv[1], l * 16, sKV, a * 16 * 4096 + 4096);
  kv_issue(kv[2], l * 16, sKV, a * 16 * 4096 + 8192);
  const float qn = fmaxf(fmaxf(qn4[0], qn4[1]), fmaxf(qn4[2], qn4[3]));
  const float bound = __builtin_sqrtf(qn * kn) * 1.02f + fabsf(__builtin_amdgcn_logf(vm));
  f32x16 o[4];
#pragma unroll
  for (int u = 0; u < 4; u++) zero16(o[u]);
  float m[4] = {0.f, 0.f, 0.f, 0.f};
  const bool fast = __builtin_amdgcn_readfirstlane(bound <= 100.0f);
  __builtin_amdgcn_s_setprio(APRIO2);
  if (fast) attn_fast(o, q, kv, sKV, a * 16 * 4096, l);
  else { kv_wait<0>(kv[0]); kv_wait<0>(kv[1]); kv_wait<0>(kv[2]); attn_safe(o, m, q, sKV, a * 16 * 4096, l); }
  cnt_spin(lds + C3, 4);
#pragma unroll
  for (int u = 1; u < 4; u++) {
    const unsigned dst = L_RED + (((a + u) & 3) * 3 + (u - 1)) * 4096 + l * 16;
#pragma unroll
    for (int g = 0; g < 4; g++) *(LAS f32x4*)(lds + dst + g * 1024) = f32x4{o[u][4 * g], o[u][4 * g + 1], o[u][4 * g + 2], o[u][4 * g + 3]};
  }
  if (!fast) {
#pragma unroll
    for (int u = 0; u < 4; u++) *(LAS float*)(lds + L_M + (a * 4 + ((a + u) & 3)) * 256 + l * 4) = m[u];
  }
  grp_arrive(lds + CA);
  f32x16 t = o[0];
  cnt_spin(lds + CA, 4);
  if (fast) {
    ldsp pr = lds + L_RED + a * 12288 + l * 16;
    add3(t, pr, pr + 4096, pr + 8192);
  } else {
    float mw[4];
#pragma unroll
    for (int u = 0; u < 4; u++) mw[u] = *(LAS float*)(lds + L_M + (((a - u) & 3) * 4 + a) * 256 + l * 4);
    const float M = fmaxf(fmaxf(mw[0], mw[1]), fmaxf(mw[2], mw[3]));
    const float so = __builtin_amdgcn_exp2f(mw[0] - M);
#pragma unroll
    for (int r = 0; r < 16; r++) t[r] *= so;
#pragma unroll
    for (int u = 1; u < 4; u++) {
      const float sc = __builtin_amdgcn_exp2f(mw[u] - M);
#pragma unroll
      for (int g = 0; g < 4; g++) {
        const f32x4 v = *(LAS f32x4*)(lds + L_RED + (a * 3 + (u - 1)) * 4096 + g * 1024 + l * 16);
#pragma unroll
        for (int e = 0; e < 4; e++) t[4 * g + e] = __builtin_fmaf(v[e], sc, t[4 * g + e]);
      }
    }
  }
  const float lsum = half_sum(h ? t[12] : 0.f);
  const float sc = gate * __builtin_amdgcn_rcpf(lsum);
  float y[16];
#pragma unroll
  for (int r = 0; r < 16; r++) { y[r] = t[r] * sc; if (r >= 12 && h) y[r] = 0.f; }
  u32x4 f0 = {pk(y[0], y[1]), pk(y[2], y[3]), pk(y[4], y[5]), pk(y[6], y[7])};
  u32x4 f1 = {pk(y[8], y[9]), pk(y[10], y[11]), pk(y[12], y[13]), pk(y[14], y[15])};
  const unsigned oo = WS_O + ((2 * g64 + (a & 1)) * 32 + 2 * (2 * j + (a >> 1))) * 1024;
  st16_sc1(f0, sWS, l * 16, oo); st16_sc1(f1, sWS, l * 16, oo + 1024);
}

__global__ void __launch_bounds__(512, 2) fused_kernel(const char* __restrict__ x, const char* __restrict__ wq, const char* __restrict__ wk,
                                                       const char* __restrict__ wv, const char* __restrict__ wo, const char* __restrict__ w1,
                                                       const char* __restrict__ b1, const char* __restrict__ w2, const char* __restrict__ b2,
                                                       const char* __restrict__ gw, char* __restrict__ ws, char* __restrict__ out) {
  extern __shared__ __attribute__((aligned(16))) char smem[];
  asm volatile("" :: "s"(x), "s"(wq), "s"(wk), "s"(wv), "s"(wo), "s"(w1), "s"(b1), "s"(w2), "s"(b2), "s"(gw), "s"(ws), "s"(out));
  ldsp lds = (ldsp)smem;
  const unsigned ldsb = (unsigned)(size_t)lds;
  const int b = blockIdx.x, xcd = b & 7, sl = b >> 3;
  const int g64 = 4 * xcd + (sl >> 3), j = sl & 7;
  const int w = __builtin_amdgcn_readfirstlane(threadIdx.x >> 6), l = threadIdx.x & 63;
  const srd_t sWS = mk_srd(ws, (unsigned)WS_B), sFL = mk_srd(ws + WS_FL, FL_SIZE);
  unsigned epoch;
  asm volatile("s_nop 4\n\tbuffer_load_dword %0, off, %1, 0 sc1" : "=v"(epoch) : "s"(sFL) : "memory");
  unsigned gwv = 0;
  u32x4 wq1[7];
  if (threadIdx.x < 16) *(LAS unsigned*)(lds + L_CNT + threadIdx.x * 4) = 0;
  lgkm0();
  bar();
  if (w >= 4) {
    __builtin_amdgcn_s_setprio(PPRIO);
    const int f = w - 4;
    const srd_t sX = mk_srd(x, S * D * 2);
    const unsigned tok = 16 * f + (l >> 3);
    const unsigned vx0 = (64 * g64 + tok) * 896 + 16 * ((l & 7) ^ ((tok >> 1) & 7));
    const unsigned vx1 = (64 * g64 + tok + 8) * 896 + 16 * ((l & 7) ^ (((tok + 8) >> 1) & 7));
    asm volatile("s_nop 4");
#pragma unroll
    for (int ch = 0; ch < 7; ch++) {
      dma16(ldsb + L_X + (2 * f * 7 + ch) * 1024, vx0, sX, ch * 128);
      dma16(ldsb + L_X + ((2 * f + 1) * 7 + ch) * 1024, vx1, sX, ch * 128);
    }
    if (j < 4) {
      const srd_t sK = mk_srd(j < 2 ? wk : wv, D * DH * 2);
      asm volatile("s_nop 4");
#pragma unroll
      for (int i = 0; i < 6; i++) dma16(ldsb + L_WKV + (f + 4 * i) * 1024, l * 16, sK, (f + 4 * i) * 1024);
      if (f == 0 && l < 32) dma16(ldsb + L_WKV + 24 * 1024, l * 16, sK, 24 * 1024);
    }
    const int T0 = f < 3 ? 2 * (3 * j + f) : 48 + j;
    asm volatile("s_nop 4");
    if (f < 3 || l < 16) dma4(ldsb + L_B1 + f * 256, l * 4, mk_srd(b1, F * 2), 64 * T0);
    if (f == 0 && l < 32) dma4(ldsb + L_B2, l * 4, mk_srd(b2, D * 2), 128 * j);
  } else {
    __builtin_amdgcn_s_setprio(APRIO);
    {
      const unsigned oo = WS_O + ((2 * g64 + (w & 1)) * 32 + 2 * (2 * j + (w >> 1))) * 1024;
      const u32x4 mk = {SENT, SENT, SENT, SENT};
      st16_sc1(mk, sWS, l * 16, oo); st16_sc1(mk, sWS, l * 16, oo + 1024);
    }
    const srd_t sG = mk_srd(gw, D * 2), sQ = mk_srd(wq, D * D * 2);
    asm volatile("s_nop 4\n\tbuffer_load_dword %0, %1, %2, 0 offen" : "=v"(gwv) : "v"((224 * (l >> 5) + 56 * w + 2 * (l & 31)) * 2), "s"(sG) : "memory");
    const unsigned vq = (56 * w + (l >> 3)) * 896 + 112 * j + 16 * ((l & 7) ^ (((l >> 4) & 1) << 2));
#pragma unroll
    for (int i = 0; i < 7; i++) dma16(ldsb + L_WQ + (56 * w + 8 * i) * 128, vq, sQ, i * 8 * 896);
    __builtin_amdgcn_s_setprio(WQ1PRIO);
#pragma unroll
    for (int i = 0; i < 7; i++)
      asm volatile("buffer_load_dwordx4 %0, %1, %2, %3 offen" : "=v"(wq1[i]) : "v"(vq), "s"(sQ), "s"((224 + i * 8) * 896) : "memory");
    __builtin_amdgcn_s_setprio(APRIO);
  }
  Ptrs P = {x, wq, wk, wv, wo, w1, b1, w2, b2, gw, ws, out};
  if (w < 4) {
    wait_vm<14>();
    asm volatile("" : "+v"(epoch));
    const unsigned tag = __builtin_amdgcn_readfirstlane(epoch) + 1u;
    a_wave(P, lds, ldsb, g64, j, w, l, tag, gwv, wq1, sWS, sFL);
  } else f_wave(P, lds, ldsb, g64, j, w - 4, l, epoch, sWS, sFL);
}

extern "C" void kernel_launch(void* const* d_in, const int* in_sizes, int n_in, void* d_out, int out_size, void* d_ws, size_t ws_size,
                              hipStream_t stream) {
  (void)hipFuncSetAttribute((const void*)fused_kernel, hipFuncAttributeMaxDynamicSharedMemorySize, L_TOTAL);
  fused_kernel<<<256, 512, L_TOTAL, stream>>>((const char*)d_in[0], (const char*)d_in[1], (const char*)d_in[2], (const char*)d_in[3],
                                              (const char*)d_in[4], (const char*)d_in[6], (const char*)d_in[7], (const char*)d_in[8],
                                              (const char*)d_in[9], (const char*)d_in[10], (char*)d_ws, (char*)d_out);
}
```

```cpp
#include <hip/hip_runtime.h>
#include <hip/hip_bf16.h>
#include <stdint.h>

typedef __attribute__((ext_vector_type(8))) __bf16 bf16x8;
typedef __attribute__((ext_vector_type(4))) __bf16 bf16x4;
typedef __attribute__((ext_vector_type(2))) __bf16 bf16x2;
typedef __attribute__((ext_vector_type(16))) float f32x16;
typedef __attribute__((ext_vector_type(4))) float f32x4;
typedef __attribute__((ext_vector_type(2))) float f32x2;
typedef __attribute__((ext_vector_type(4))) short s16x4;
typedef __attribute__((ext_vector_type(8))) short s16x8;
typedef __attribute__((ext_vector_type(4))) unsigned u32x4;
typedef __attribute__((ext_vector_type(2))) unsigned u32x2;
#define LAS __attribute__((address_space(3)))
#define GAS __attribute__((address_space(1)))
typedef __amdgpu_buffer_rsrc_t srd_t;
typedef LAS char* ldsp;

constexpr int S = 2048, D = 448, NH = 16, DH = 28, F = 1792;
constexpr size_t WS_KV = 0;
constexpr size_t WS_Q  = WS_KV + 64 * 4 * 1024;
constexpr size_t WS_H  = WS_Q + 16 * 64 * 2 * 1024;
constexpr size_t WS_O  = WS_H + 64 * 112 * 1024;
constexpr size_t WS_G  = WS_O + 64 * 32 * 1024;
constexpr size_t WS_QN = WS_G + 2048 * 4;
constexpr size_t WS_KN = WS_QN + 16 * 64 * 4;
constexpr size_t WS_VM = WS_KN + 64 * 4;
constexpr size_t WS_B  = WS_VM + 64 * 4;

#define SRD_W3 0x00020000
__device__ __forceinline__ srd_t mk_srd(const void* p, unsigned bytes) {
  return __builtin_amdgcn_make_buffer_rsrc((void*)p, (short)0, (int)bytes, SRD_W3);
}
__device__ __forceinline__ void dma16(unsigned m0v, unsigned voff, srd_t srd, unsigned soff) {
  asm volatile("s_mov_b32 m0, %0\n\ts_nop 0\n\tbuffer_load_dwordx4 %1, %2, %3 offen lds"
               :: "s"(m0v), "v"(voff), "s"(srd), "s"(soff) : "memory");
}
__device__ __forceinline__ void dma4(unsigned m0v, unsigned voff, srd_t srd, unsigned soff) {
  asm volatile("s_mov_b32 m0, %0\n\ts_nop 0\n\tbuffer_load_dword %1, %2, %3 offen lds"
               :: "s"(m0v), "v"(voff), "s"(srd), "s"(soff) : "memory");
}
template <int N> __device__ __forceinline__ void wait_vm() { asm volatile("s_waitcnt vmcnt(%0)" :: "n"(N) : "memory"); }
__device__ __forceinline__ void bar() { asm volatile("s_barrier" ::: "memory"); }
__device__ __forceinline__ bf16x8 rd128(ldsp p) { return *(LAS bf16x8*)p; }
__device__ __forceinline__ bf16x8 rdtr(ldsp p0, ldsp p1) {
  s16x4 a = __builtin_amdgcn_ds_read_tr16_b64_v4i16((LAS s16x4*)p0);
  s16x4 b = __builtin_amdgcn_ds_read_tr16_b64_v4i16((LAS s16x4*)p1);
  s16x8 r = __builtin_shufflevector(a, b, 0, 1, 2, 3, 4, 5, 6, 7);
  return __builtin_bit_cast(bf16x8, r);
}
__device__ __forceinline__ f32x16 mfma(bf16x8 a, bf16x8 b, f32x16 c) {
  return __builtin_amdgcn_mfma_f32_32x32x16_bf16(a, b, c, 0, 0, 0);
}
__device__ __forceinline__ unsigned pk(float lo, float hi) {
  f32x2 v = {lo, hi};
  bf16x2 r = __builtin_convertvector(v, bf16x2);
  return __builtin_bit_cast(unsigned, r);
}
__device__ __forceinline__ float bflo(unsigned u) { return __uint_as_float(u << 16); }
__device__ __forceinline__ float bfhi(unsigned u) { return __uint_as_float(u & 0xffff0000u); }
template <int CTRL> __device__ __forceinline__ float dpp_f(float v) {
  return __int_as_float(__builtin_amdgcn_update_dpp(0, __float_as_int(v), CTRL, 0xf, 0xf, true));
}
__device__ __forceinline__ float wave_max_nn(float v) {
  v = fmaxf(v, dpp_f<0xB1>(v));
  v = fmaxf(v, dpp_f<0x4E>(v));
  v = fmaxf(v, dpp_f<0x141>(v));
  v = fmaxf(v, dpp_f<0x140>(v));
  unsigned a = __builtin_amdgcn_readlane(__float_as_uint(v), 0), b = __builtin_amdgcn_readlane(__float_as_uint(v), 16);
  unsigned c = __builtin_amdgcn_readlane(__float_as_uint(v), 32), d = __builtin_amdgcn_readlane(__float_as_uint(v), 48);
  a = a > b ? a : b; c = c > d ? c : d; a = a > c ? a : c;
  return __uint_as_float(a);
}
__device__ __forceinline__ float half_sum(float s) {
  auto r = __builtin_amdgcn_permlane32_swap(__float_as_uint(s), __float_as_uint(s), false, false);
  return __uint_as_float(r[0]) + __uint_as_float(r[1]);
}


#define GSB() __builtin_amdgcn_sched_barrier(0)
__device__ __forceinline__ u32x4 gelu8(const f32x16& a, int r0, f32x2 c1, f32x2 c2) {
  f32x2 x[4], p[4], e[4];
#pragma unroll
  for (int i = 0; i < 4; i++) { x[i] = f32x2{a[r0 + 2 * i], a[r0 + 2 * i + 1]}; p[i] = x[i] * x[i]; }
#pragma unroll
  for (int i = 0; i < 4; i++) p[i] = __builtin_elementwise_fma(p[i], c2, c1);
#pragma unroll
  for (int i = 0; i < 4; i++) p[i] = p[i] * x[i];
  GSB();
#pragma unroll
  for (int i = 0; i < 4; i++) { e[i][0] = __builtin_amdgcn_exp2f(p[i][0]); e[i][1] = __builtin_amdgcn_exp2f(p[i][1]); }
  GSB();
#pragma unroll
  for (int i = 0; i < 4; i++) e[i] = e[i] + f32x2{1.0f, 1.0f};
  GSB();
#pragma unroll
  for (int i = 0; i < 4; i++) { e[i][0] = __builtin_amdgcn_rcpf(e[i][0]); e[i][1] = __builtin_amdgcn_rcpf(e[i][1]); }
  GSB();
  u32x4 r;
#pragma unroll
  for (int i = 0; i < 4; i++) { x[i] = x[i] * e[i]; r[i] = pk(x[i][0], x[i][1]); }
  GSB();
  return r;
}


#define SB() __builtin_amdgcn_sched_barrier(0)
#define PIN(x) asm volatile("" : "+v"(x))
__device__ __forceinline__ void ld16_sc1(u32x4& d, unsigned voff, srd_t srd, unsigned soff) {
  asm volatile("buffer_load_dwordx4 %0, %1, %2, %3 offen sc1" : "=v"(d) : "v"(voff), "s"(srd), "s"(soff) : "memory");
}
template <int OFF> __device__ __forceinline__ void ld16o_sc1(u32x4& d, unsigned voff, srd_t srd, unsigned soff) {
  asm volatile("buffer_load_dwordx4 %0, %1, %2, %3 offen offset:%4 sc1" : "=v"(d) : "v"(voff), "s"(srd), "s"(soff), "n"(OFF) : "memory");
}
struct KVT { u32x4 k0, k1, v0, v1; };
__device__ __forceinline__ void kv_issue(KVT& t, unsigned voff, srd_t srd, unsigned soff) {
  ld16o_sc1<0>(t.k0, voff, srd, soff); ld16o_sc1<1024>(t.k1, voff, srd, soff);
  ld16o_sc1<2048>(t.v0, voff, srd, soff); ld16o_sc1<3072>(t.v1, voff, srd, soff);
}
template <int N> __device__ __forceinline__ void kv_wait(KVT& t) {
  asm volatile("s_waitcnt vmcnt(%4)" : "+v"(t.k0), "+v"(t.k1), "+v"(t.v0), "+v"(t.v1) : "n"(N) : "memory");
}
__device__ __forceinline__ bf16x8 asb(u32x4 v) { return __builtin_bit_cast(bf16x8, v); }
__device__ __forceinline__ f32x16 qk(const KVT& t, const bf16x8 (&q)[2]) {
  f32x16 z;
#pragma unroll
  for (int r = 0; r < 16; r++) z[r] = 0.f;
  z = mfma(asb(t.k0), q[0], z);
  return mfma(asb(t.k1), q[1], z);
}
struct Carry { float e[4]; unsigned p0, p1; };
#define EX(x) __builtin_amdgcn_exp2f(x)
template <bool has_prev, bool has_next>
__device__ __forceinline__ void attn_unit(f32x16& s, f32x16& o, f32x16& oprev, Carry& c, const u32x4& v0, const u32x4& v1prev,
                                          const u32x4& k0n, const u32x4& k1n, const bf16x8 (&qn)[2]) {
  f32x16 sn;
#pragma unroll
  for (int r = 0; r < 16; r++) sn[r] = 0.f;
  if (has_next) { sn = mfma(asb(k0n), qn[0], sn); PIN(sn); }
  SB();
  u32x4 pp;
  if (has_prev) { pp[0] = c.p0; pp[1] = c.p1; pp[2] = pk(c.e[0], c.e[1]); pp[3] = pk(c.e[2], c.e[3]); }
  float e0 = EX(s[0]), e1 = EX(s[1]), e2 = EX(s[2]), e3 = EX(s[3]);
  PIN(e0); PIN(e1); PIN(e2); PIN(e3);
  if (has_prev) PIN(pp);
  SB();
  if (has_prev) { oprev = mfma(asb(v1prev), asb(pp), oprev); PIN(oprev); }
  SB();
  float e4 = EX(s[4]), e5 = EX(s[5]), e6 = EX(s[6]), e7 = EX(s[7]);
  u32x4 p;
  p[0] = pk(e0, e1); p[1] = pk(e2, e3);
  PIN(e4); PIN(e5); PIN(e6); PIN(e7); PIN(p[0]); PIN(p[1]);
  SB();
  if (has_next) { sn = mfma(asb(k1n), qn[1], sn); PIN(sn); }
  SB();
  p[2] = pk(e4, e5); p[3] = pk(e6, e7);
  float e8 = EX(s[8]), e9 = EX(s[9]), e10 = EX(s[10]), e11 = EX(s[11]);
  PIN(p); PIN(e8); PIN(e9); PIN(e10); PIN(e11);
  SB();
  o = mfma(asb(v0), asb(p), o); PIN(o);
  SB();
  c.e[0] = EX(s[12]); c.e[1] = EX(s[13]); c.e[2] = EX(s[14]); c.e[3] = EX(s[15]);
  c.p0 = pk(e8, e9); c.p1 = pk(e10, e11);
  PIN(c.e[0]); PIN(c.e[1]); PIN(c.e[2]); PIN(c.e[3]); PIN(c.p0); PIN(c.p1);
  s = sn;
  SB();
}
template <int MODE>
__device__ __forceinline__ void attn_block(KVT (&kv)[4], f32x16& s, f32x16 (&o)[4], Carry& c, const bf16x8 (&q)[4][2], unsigned voff, srd_t srd,
                                           unsigned soff) {
#pragma unroll
  for (int t = 0; t < 4; t++) {
    KVT& cur = kv[t]; KVT& prv = kv[(t + 3) & 3]; KVT& nxt = kv[(t + 1) & 3];
    if (MODE == 1 && t == 0) attn_unit<false, true>(s, o[0], o[3], c, cur.v0, prv.v1, cur.k0, cur.k1, q[1]);
    else attn_unit<true, true>(s, o[0], o[3], c, cur.v0, prv.v1, cur.k0, cur.k1, q[1]);
    if (MODE != 2 || t == 0) kv_issue(prv, voff, srd, soff + (t + 3) * 4096);
    SB();
    attn_unit<true, true>(s, o[1], o[0], c, cur.v0, cur.v1, cur.k0, cur.k1, q[2]);
    attn_unit<true, true>(s, o[2], o[1], c, cur.v0, cur.v1, cur.k0, cur.k1, q[3]);
    if (MODE != 2 || t == 0) kv_wait<8>(nxt);
    else if (t == 1) kv_wait<4>(nxt);
    else if (t == 2) kv_wait<0>(nxt);
    SB();
    if (MODE == 2 && t == 3) attn_unit<true, false>(s, o[3], o[2], c, cur.v0, cur.v1, cur.k0, cur.k1, q[0]);
    else attn_unit<true, true>(s, o[3], o[2], c, cur.v0, cur.v1, nxt.k0, nxt.k1, q[0]);
  }
  if (MODE == 2) {
    u32x4 pp = {c.p0, c.p1, pk(c.e[0], c.e[1]), pk(c.e[2], c.e[3])};
    o[3] = mfma(asb(kv[3].v1), asb(pp), o[3]);
  }
}
__device__ __forceinline__ void attn_fast(f32x16 (&o)[4], const bf16x8 (&q)[4][2], KVT (&kv)[4], srd_t srd, unsigned soff0, int l) {
  const unsigned voff = l * 16;
  kv[3].v1 = kv[3].v0 = kv[3].k0 = kv[3].k1 = u32x4{0, 0, 0, 0};
  kv_wait<8>(kv[0]);
  f32x16 s = qk(kv[0], q[0]);
  Carry c;
  attn_block<1>(kv, s, o, c, q, voff, srd, soff0);
#pragma unroll 1
  for (int it = 1; it < 3; it++) {
    attn_block<0>(kv, s, o, c, q, voff, srd, soff0 + it * 16384);
  }
  attn_block<2>(kv, s, o, c, q, voff, srd, soff0 + 3 * 16384);
}
__device__ __forceinline__ void attn_safe(f32x16 (&o)[4], float (&m)[4], const bf16x8 (&q)[4][2], srd_t srd, unsigned soff0, int l) {
  const unsigned voff = l * 16;
#pragma unroll
  for (int u = 0; u < 4; u++) m[u] = -3.0e38f;
#pragma unroll 1
  for (int t = 0; t < 16; t++) {
    KVT kv;
    kv_issue(kv, voff, srd, soff0 + t * 4096);
    kv_wait<0>(kv);
#pragma unroll
    for (int u = 0; u < 4; u++) {
      f32x16 s = qk(kv, q[u]);
      float mx = s[0];
#pragma unroll
      for (int r = 1; r < 16; r++) mx = fmaxf(mx, s[r]);
      auto sw = __builtin_amdgcn_permlane32_swap(__float_as_uint(mx), __float_as_uint(mx), false, false);
      mx = fmaxf(__uint_as_float(sw[0]), __uint_as_float(sw[1]));
      const float mn = fmaxf(m[u], mx);
      const float al = __builtin_amdgcn_exp2f(m[u] - mn);
      m[u] = mn;
      u32x4 p0, p1;
#pragma unroll
      for (int i = 0; i < 4; i++) {
        p0[i] = pk(__builtin_amdgcn_exp2f(s[2 * i] - mn), __builtin_amdgcn_exp2f(s[2 * i + 1] - mn));
        p1[i] = pk(__builtin_amdgcn_exp2f(s[8 + 2 * i] - mn), __builtin_amdgcn_exp2f(s[8 + 2 * i + 1] - mn));
      }
#pragma unroll
      for (int r = 0; r < 16; r++) o[u][r] *= al;
      o[u] = mfma(asb(kv.v0), asb(p0), o[u]);
      o[u] = mfma(asb(kv.v1), asb(p1), o[u]);
    }
  }
}

__device__ __forceinline__ void grp_arrive(ldsp cnt) {
  asm volatile("s_waitcnt lgkmcnt(0)" ::: "memory");
  if ((threadIdx.x & 63) == 0) __hip_atomic_fetch_add((LAS unsigned*)cnt, 1u, __ATOMIC_RELAXED, __HIP_MEMORY_SCOPE_WORKGROUP);
}
__device__ __forceinline__ void grp_wait(ldsp cnt, unsigned target) {
  while (__builtin_amdgcn_readfirstlane(*(volatile LAS unsigned*)cnt) < target) __builtin_amdgcn_s_sleep(1);
  asm volatile("" ::: "memory");
}

constexpr size_t WS_FL = WS_B;
constexpr unsigned FL_KV = 4096, FL_H = 36864, FL_HG = 102400, FL_SIZE = 106496;
constexpr int L_X = 0, L_WQ = 57344, L_WKV = L_WQ + 28672, L_RING = L_WKV + 25088, L_QX = L_RING + 3 * 14336, L_MISC = L_QX + 8192;
constexpr int L_B1 = L_MISC, L_B2 = L_B1 + 896, L_GATE = L_B2 + 128, L_QN = L_GATE + 256, L_CNT = L_QN + 16;
__device__ __forceinline__ unsigned ring_off(int f) { return f < 3 ? L_RING + f * 14336 : L_WKV; }
constexpr int L_TOTAL = 163840;
constexpr int C0 = L_CNT, C1 = L_CNT + 4, C2 = L_CNT + 8, C3 = L_CNT + 12, CH = L_CNT + 16, CA = L_CNT + 20, CO = L_CNT + 24, CF = L_CNT + 28, CX = L_CNT + 32;
constexpr int L_RED = L_X, L_M = L_X + 49152;
constexpr unsigned SPIN_LIMIT = 100000;
constexpr int AUX_SC1 = 16;
constexpr int FPRIO = 0;
constexpr int APRIO = 0;
constexpr int APRIO2 = 0;
constexpr int PPRIO = 3;
constexpr int EARLY = 0;
constexpr int WQ1PRIO = 0;
constexpr int HRING = 6;
constexpr int GPRIO = 2;
constexpr int F2PRIO = 0;

__device__ __forceinline__ void st16_sc1(u32x4 v, srd_t srd, unsigned voff, unsigned soff) {
  __builtin_amdgcn_raw_buffer_store_b128(v, srd, voff + soff, 0, AUX_SC1);
}
__device__ __forceinline__ void lgkm0() { asm volatile("s_waitcnt lgkmcnt(0)" ::: "memory"); }
__device__ __forceinline__ void cnt_wait(ldsp p, unsigned target) {
#pragma unroll 1
  for (unsigned it = 0; it < (1u << 22); it++) {
    if (__builtin_amdgcn_readfirstlane(*(volatile LAS unsigned*)p) >= target) break;
    __builtin_amdgcn_s_sleep(1);
  }
  asm volatile("" ::: "memory");
}
__device__ __forceinline__ void cnt_spin(ldsp p, unsigned target) {
#pragma unroll 1
  for (unsigned it = 0; it < (1u << 24); it++)
    if (__builtin_amdgcn_readfirstlane(*(volatile LAS unsigned*)p) >= target) break;
  asm volatile("" ::: "memory");
}
__device__ __forceinline__ bool cnt_last(ldsp p, unsigned n) {
  unsigned old = 0;
  if ((threadIdx.x & 63) == 0) old = __hip_atomic_fetch_add((LAS unsigned*)p, 1u, __ATOMIC_RELAXED, __HIP_MEMORY_SCOPE_WORKGROUP);
  return __builtin_amdgcn_readfirstlane(old) == n - 1;
}
__device__ __forceinline__ void poll4(srd_t srd, unsigned voff, unsigned tag) {
#pragma unroll 1
  for (unsigned it = 0; it < SPIN_LIMIT; it++) {
    unsigned v;
    asm volatile("buffer_load_dword %0, %1, %2, 0 offen sc1\n\ts_waitcnt vmcnt(0)" : "=v"(v) : "v"(voff), "s"(srd) : "memory");
    if (__builtin_amdgcn_ballot_w64(v != tag) == 0) break;
    __builtin_amdgcn_s_sleep(2);
  }
}
__device__ __forceinline__ u32x2 poll8(srd_t srd, unsigned voff, unsigned tag) {
  u32x2 v;
#pragma unroll 1
  for (unsigned it = 0; it < SPIN_LIMIT; it++) {
    asm volatile("buffer_load_dwordx2 %0, %1, %2, 0 offen sc1\n\ts_waitcnt vmcnt(0)" : "=v"(v) : "v"(voff), "s"(srd) : "memory");
    if (__builtin_amdgcn_ballot_w64(v[1] != tag) == 0) break;
    __builtin_amdgcn_s_sleep(2);
  }
  return v;
}

template <bool SWAP, int WSTEP, int WR2, int KS0, int NB, int SIG = -1>
__device__ __forceinline__ void proj_chain(f32x16& acc, ldsp lds, unsigned wb, unsigned xl) {
  bf16x8 w[2][7], x[2][7];
  ldsp wp = lds + wb;
  ldsp xp[4];
#pragma unroll
  for (int i = 0; i < 4; i++) xp[i] = lds + (xl ^ (32u * i));
#pragma unroll
  for (int i = 0; i < 7; i++) { w[0][i] = rdtr(wp + i * WSTEP, wp + i * WSTEP + WR2); x[0][i] = rd128(xp[(KS0 + i) & 3] + ((KS0 + i) >> 2) * 1024); }
  SB();
#pragma unroll
  for (int b = 0; b < NB; b++) {
#pragma unroll
    for (int i = 0; i < 7; i++) {
      const int ks = 7 * b + i;
      acc = SWAP ? mfma(x[b & 1][i], w[b & 1][i], acc) : mfma(w[b & 1][i], x[b & 1][i], acc);
      if (b < NB - 1) {
        const int kn = ks + 7;
        w[(b + 1) & 1][i] = rdtr(wp + kn * WSTEP, wp + kn * WSTEP + WR2); x[(b + 1) & 1][i] = rd128(xp[(KS0 + kn) & 3] + ((KS0 + kn) >> 2) * 1024);
      }
      SB();
    }
    if (SIG >= 0 && b == NB - 2) { grp_arrive(lds + SIG); SB(); }
  }
}

template <int I> struct IC { static constexpr int v = I; };
template <int I, int N, class Fn> __device__ __forceinline__ void static_for(Fn&& fn) {
  if constexpr (I < N) { fn(IC<I>{}); static_for<I + 1, N>(fn); }
}
template <int OFF> __device__ __forceinline__ void set_m0(unsigned base) {
  asm volatile("s_add_u32 m0, %0, %1" :: "s"(base), "n"(OFF) : "scc", "memory");
}
__device__ __forceinline__ void dma_go(unsigned voff, srd_t srd, unsigned soff) {
  asm volatile("buffer_load_dwordx4 %0, %1, %2 offen lds" :: "v"(voff), "s"(srd), "s"(soff) : "memory");
}

struct FCtx {
  ldsp lds; unsigned ldsb;
  unsigned ring;
  unsigned ringb;
  unsigned la0, la1;
  unsigned xl;
  unsigned vW1, vW1b, vW2, vW2b;
  srd_t sW1, sW2, sWO;
  unsigned w2o, woo;
  int f, j;
};

template <int NSUB> struct F1Frag { bf16x8 a[NSUB]; bf16x8 b[2]; };
template <int NSUB> __device__ __forceinline__ void f1_issue_w1(const FCtx& c, unsigned ldsdst, int ks) {
  dma16(c.ldsb + ldsdst, c.vW1, c.sW1, ks * (16 * 3584));
  if (NSUB == 2) dma16(c.ldsb + ldsdst + 1024, c.vW1, c.sW1, ks * (16 * 3584) + 8 * 3584);
}
template <int NSUB> __device__ __forceinline__ void f1_load(F1Frag<NSUB>& fr, ldsp a0, ldsp a1, ldsp xp) {
  constexpr int R2 = NSUB == 2 ? 512 : 256;
  fr.a[0] = rdtr(a0, a0 + R2);
  if (NSUB == 2) fr.a[NSUB - 1] = rdtr(a1, a1 + R2);
  fr.b[0] = rd128(xp); fr.b[1] = rd128(xp + 28672);
}
template <int NSUB, int E>
__device__ __forceinline__ void f1_step(const FCtx& c, F1Frag<NSUB>& cur, F1Frag<NSUB>& nxt, f32x16 (&acc)[2][2], ldsp pa0, ldsp pa1,
                                        const ldsp (&xp)[4], unsigned& soff) {
  constexpr int R2 = NSUB == 2 ? 512 : 256;
  constexpr int SL = (E % 7) * 2048, SN = ((E + 1) % 7) * 2048;
  constexpr bool W1 = E < 21;
  constexpr int XO = ((E + 1) >> 2) * 1024;
  wait_vm<NSUB == 2 ? 10 : 5>();
  if (E == 21) soff = c.w2o;
  SB();
  acc[0][0] = mfma(cur.a[0], cur.b[0], acc[0][0]);
  SB();
  if (E < 27) { nxt.a[0] = rdtr(pa0 + SN, pa0 + SN + R2); nxt.b[0] = rd128(xp[(E + 1) & 3] + XO); }
  SB();
  acc[0][1] = mfma(cur.a[0], cur.b[1], acc[0][1]);
  SB();
  if (E < 27) { nxt.b[1] = rd128(xp[(E + 1) & 3] + XO + 28672); if (NSUB == 2) nxt.a[NSUB - 1] = rdtr(pa1 + SN, pa1 + SN + R2); }
  set_m0<SL>(c.ringb);
  SB();
  if (NSUB == 2) { acc[1][0] = mfma(cur.a[NSUB - 1], cur.b[0], acc[1][0]); } else { asm volatile("s_waitcnt lgkmcnt(%0)" :: "n"(E < 27 ? 4 : 0) : "memory"); }
  SB();
  dma_go(W1 ? c.vW1 : c.vW2, W1 ? c.sW1 : c.sW2, soff);
  if (NSUB == 2 || !W1) set_m0<SL + 1024>(c.ringb);
  SB();
  if (NSUB == 2) { acc[1][1] = mfma(cur.a[NSUB - 1], cur.b[1], acc[1][1]); } else if (!W1) asm volatile("s_nop 0");
  SB();
  if (NSUB == 2 || !W1) dma_go(W1 ? c.vW1b : c.vW2b, W1 ? c.sW1 : c.sW2, soff);
  soff += W1 ? 16 * 3584 : 16 * 896;
  SB();
}
template <int NSUB> __device__ __forceinline__ void ffn1(FCtx& c, f32x16 (&acc)[2][2], char* ws) {
  F1Frag<NSUB> f0, f1;
  ldsp xp[4];
#pragma unroll
  for (int i = 0; i < 4; i++) xp[i] = c.lds + (c.xl ^ (32u * i));
  unsigned soff = 7 * (16 * 3584);
  ldsp pa0 = c.lds + c.la0, pa1 = c.lds + c.la1;
  wait_vm<NSUB == 2 ? 12 : 6>();
  f1_load<NSUB>(f0, pa0, pa1, xp[0]);
  static_for<0, 28>([&](auto ic) {
    constexpr int E = decltype(ic)::v;
    if constexpr ((E & 1) == 0) f1_step<NSUB, E>(c, f0, f1, acc, pa0, pa1, xp, soff);
    else f1_step<NSUB, E>(c, f1, f0, acc, pa0, pa1, xp, soff);
  });
}

struct AFr { bf16x8 a[2]; };
struct HB { u32x4 t0, t1; };
template <int N> __device__ __forceinline__ void hb_wait(HB& h) { asm volatile("s_waitcnt vmcnt(%2)" : "+v"(h.t0), "+v"(h.t1) : "n"(N) : "memory"); }
constexpr int HR = HRING;
constexpr int f2_nd(int s) { return s + 7 < 28 ? 2 : 0; }
constexpr int f2_nh(int s) { return s + HR < 28 ? 2 : 0; }
constexpr int f2_sum(int a, int b) { int r = 0; for (int s = a; s < b; s++) r += f2_nd(s) + f2_nh(s); return r; }
constexpr int f2_wait(int S) {
  int wh = S < HR ? 2 * (HR - 1 - S) + f2_sum(0, S) : f2_sum(S - HR + 1, S);
  if (S + 1 >= 7 && S + 1 < 28) { int wi = f2_nh(S + 1 - 7) + f2_sum(S + 1 - 6, S); wh = wi < wh ? wi : wh; }
  return wh;
}
template <int S>
__device__ __forceinline__ void f2_step(const FCtx& c, AFr& cur, AFr& nxt, HB& h, f32x16 (&acc)[2][2], ldsp pa0, ldsp pa1, unsigned& soff,
                                        unsigned hv, srd_t sH, unsigned& hs, unsigned hd) {
  constexpr int SL = (S % 7) * 2048, SN = ((S + 1) % 7) * 2048;
  constexpr bool W2 = S < 21;
  hb_wait<f2_wait(S)>(h);
  SB();
  acc[0][0] = mfma(cur.a[0], asb(h.t0), acc[0][0]);
  SB();
  if (S < 27) nxt.a[0] = rdtr(pa0 + SN, pa0 + SN + 1024);
  SB();
  acc[0][1] = mfma(cur.a[0], asb(h.t1), acc[0][1]);
  SB();
  if (S < 27) nxt.a[1] = rdtr(pa1 + SN, pa1 + SN + 1024);
  if (W2) set_m0<SL>(c.ringb);
  SB();
  acc[1][0] = mfma(cur.a[1], asb(h.t0), acc[1][0]);
  SB();
  if (W2) { dma_go(c.vW2, c.sW2, soff); set_m0<SL + 1024>(c.ringb); }
  SB();
  acc[1][1] = mfma(cur.a[1], asb(h.t1), acc[1][1]);
  SB();
  if (W2) { dma_go(c.vW2b, c.sW2, soff); soff += 16 * 896; }
  if (S + HR < 28) { ld16o_sc1<0>(h.t0, hv, sH, hs); ld16o_sc1<0>(h.t1, hv, sH, hs + hd); hs += 1024; }
  SB();
}
constexpr unsigned SENT = 0xffffffffu;
__device__ __forceinline__ unsigned umax3(unsigned a, unsigned b, unsigned c) { a = a > b ? a : b; return a > c ? a : c; }
__device__ __forceinline__ bool hb_pending(const HB& h) {
  const unsigned m = umax3(umax3(umax3(h.t0[0], h.t0[1], h.t0[2]), h.t0[3], h.t1[0]), umax3(h.t1[1], h.t1[2], h.t1[3]), 0u);
  return __builtin_amdgcn_ballot_w64(m == SENT) != 0;
}
__device__ __forceinline__ void ffn2_tail(const FCtx& c, f32x16 (&acc)[2][2], int g64, int l, srd_t sWS, float& og0, float& og1, char* ws) {
  ldsp pa0 = c.lds + c.la0, pa1 = c.lds + c.la1;
  const unsigned hv = l * 16;
  const int ft = c.f & 1;
  unsigned hs = WS_H + ((2 * g64 + ft) * 112 + 28 * c.f) * 1024;
  const unsigned hd = ft ? 0u - 112 * 1024 : 112 * 1024;
  HB hb[HR];
#pragma unroll
  for (int i = 0; i < HR; i++) { ld16_sc1(hb[i].t0, hv, sWS, hs + i * 1024); ld16_sc1(hb[i].t1, hv, sWS, hs + i * 1024 + hd); }
  hs += HR * 1024;
#pragma unroll
  for (int n = 0; n < 2; n++)
#pragma unroll
    for (int t = 0; t < 2; t++)
#pragma unroll
      for (int r = 0; r < 16; r++) acc[n][t][r] = 0.f;
  AFr a0, a1;
  a0.a[0] = rdtr(pa0, pa0 + 1024); a0.a[1] = rdtr(pa1, pa1 + 1024);
  unsigned soff = c.w2o + 7 * (16 * 896);
  static_for<0, 28>([&](auto ic) {
    constexpr int S = decltype(ic)::v;
    if constexpr ((S & 1) == 0) f2_step<S>(c, a0, a1, hb[S % HR], acc, pa0, pa1, soff, hv, sWS, hs, hd);
    else f2_step<S>(c, a1, a0, hb[S % HR], acc, pa0, pa1, soff, hv, sWS, hs, hd);
  });
  const unsigned x7 = L_WQ + c.f * 2048;
  lgkm0();
#pragma unroll
  for (int t = 0; t < 8; t++) {
    const unsigned dst = t < 7 ? c.ringb + t * 2048 : c.ldsb + x7, so = c.woo + (28 * (t >> 1) + 16 * (t & 1)) * 896;
    dma16(dst, c.vW2, c.sWO, so); dma16(dst + 1024, c.vW2b, c.sWO, so);
  }
  cnt_wait(c.lds + C2, 4);
  og0 = 1.0f - *(LAS float*)(c.lds + L_GATE + 128 * ft + (l & 31) * 4); og1 = 1.0f - *(LAS float*)(c.lds + L_GATE + 128 * (1 - ft) + (l & 31) * 4);
#pragma unroll
  for (int n = 0; n < 2; n++)
#pragma unroll
    for (int r = 0; r < 16; r++) { acc[n][0][r] *= og0; acc[n][1][r] *= og1; }
  const unsigned os = WS_O + ((2 * g64 + ft) * 32 + 8 * c.f) * 1024, od = ft ? 0u - 32768 : 32768;
  const unsigned pv = WS_O + ((2 * g64) * 32 + 8 * c.f) * 1024 + (l & 7) * 1024 + ((l >> 3) & 1) * 32768 + 1020;
#pragma unroll 1
  for (unsigned it = 0; it < SPIN_LIMIT; it++) {
    unsigned v;
    asm volatile("buffer_load_dword %0, %1, %2, 0 offen sc1\n\ts_waitcnt vmcnt(0)" : "=v"(v) : "v"(pv), "s"(sWS) : "memory");
    if (__builtin_amdgcn_ballot_w64(v == SENT) == 0) break;
    __builtin_amdgcn_s_sleep(1);
  }
  HB ob[8];
#pragma unroll
  for (int t = 0; t < 8; t++) { ld16_sc1(ob[t].t0, hv, sWS, os + t * 1024); ld16_sc1(ob[t].t1, hv, sWS, os + t * 1024 + od); }
  const unsigned d7 = x7 - c.ring;
  a0.a[0] = rdtr(pa0, pa0 + 1024); a0.a[1] = rdtr(pa1, pa1 + 1024);
  static_for<0, 8>([&](auto ic) {
    constexpr int T = decltype(ic)::v;
    constexpr int SN = (T + 1) * 2048;
    AFr& cur = (T & 1) ? a1 : a0; AFr& nxt = (T & 1) ? a0 : a1;
    if (T < 6) { nxt.a[0] = rdtr(pa0 + SN, pa0 + SN + 1024); nxt.a[1] = rdtr(pa1 + SN, pa1 + SN + 1024); }
    if (T == 6) { nxt.a[0] = rdtr(pa0 + d7, pa0 + d7 + 1024); nxt.a[1] = rdtr(pa1 + d7, pa1 + d7 + 1024); }
    hb_wait<14 - 2 * T>(ob[T]);
    if (hb_pending(ob[T])) {
#pragma unroll 1
      for (unsigned it = 0; it < SPIN_LIMIT; it++) {
        ld16_sc1(ob[T].t0, hv, sWS, os + T * 1024); ld16_sc1(ob[T].t1, hv, sWS, os + T * 1024 + od);
        hb_wait<0>(ob[T]);
        if (!hb_pending(ob[T])) break;
      }
    }
    acc[0][0] = mfma(cur.a[0], asb(ob[T].t0), acc[0][0]); acc[0][1] = mfma(cur.a[0], asb(ob[T].t1), acc[0][1]);
    acc[1][0] = mfma(cur.a[1], asb(ob[T].t0), acc[1][0]); acc[1][1] = mfma(cur.a[1], asb(ob[T].t1), acc[1][1]);
    SB();
  });
}

__device__ __forceinline__ void h_arrive(ldsp lds, srd_t sFL, int g64, int j, int l, unsigned tag) {
  if (cnt_last(lds + CH, 8)) { if (l < 8) __builtin_amdgcn_raw_buffer_store_b32(tag, sFL, l < 7 ? FL_H + (g64 * 8 + l) * 128 + j * 4 : FL_HG + (g64 * 8 + j) * 4, 0, AUX_SC1); }
}
__device__ __forceinline__ void add3(f32x16& t, ldsp p0, ldsp p1, ldsp p2) {
  f32x4 v[12];
#pragma unroll
  for (int g = 0; g < 4; g++) { v[g] = *(LAS f32x4*)(p0 + g * 1024); v[4 + g] = *(LAS f32x4*)(p1 + g * 1024); v[8 + g] = *(LAS f32x4*)(p2 + g * 1024); }
  SB();
#pragma unroll
  for (int i = 0; i < 12; i++) {
    const int g = i & 3;
    const f32x2 lo = f32x2{t[4 * g], t[4 * g + 1]} + f32x2{v[i][0], v[i][1]}, hi = f32x2{t[4 * g + 2], t[4 * g + 3]} + f32x2{v[i][2], v[i][3]};
    t[4 * g] = lo[0]; t[4 * g + 1] = lo[1]; t[4 * g + 2] = hi[0]; t[4 * g + 3] = hi[1];
  }
}
struct Ptrs { const char *x, *wq, *wk, *wv, *wo, *w1, *b1, *w2, *b2, *gw; char *ws, *out; };

__device__ __forceinline__ void zero16(f32x16& a) {
#pragma unroll
  for (int r = 0; r < 16; r++) a[r] = 0.f;
}

__device__ __forceinline__ void f_wave(const Ptrs& P, ldsp lds, unsigned ldsb, int g64, int j, int sl, int f, int l, unsigned epoch, srd_t sWS, srd_t sFL) {
  const int h = l >> 5;
  FCtx c;
  c.lds = lds; c.ldsb = ldsb; c.f = f; c.j = j;
  c.ring = ring_off(f); c.ringb = ldsb + c.ring;
  c.w2o = (unsigned)(28 * f) * (16 * 896); c.woo = (unsigned)(28 * 4 * f) * 896;
  c.sW1 = mk_srd(P.w1, D * F * 2); c.sW2 = mk_srd(P.w2, D * F * 2); c.sWO = mk_srd(P.wo, D * D * 2);
  const unsigned q4 = (l & 15) >> 2, g1 = (l >> 4) & 1;
  const unsigned swz = 16 * ((l & 7) ^ (((l >> 4) & 1) << 2));
  c.vW2 = (l >> 3) * 896 + 128 * j + swz; c.vW2b = c.vW2 + 8 * 896;
  const unsigned cpart = 16 * ((2 * g1 + ((l & 3) >> 1)) ^ (((q4 >> 1) & 1) << 2)) + 8 * (l & 1);
  c.xl = L_X + ((l & 31) >> 3) * 7168 + (l & 7) * 128 + 16 * (h ^ (((l & 31) >> 1) & 7));
  char* ws = P.ws;
  f32x16 acc[2][2];
  const int nsub = f < 3 ? 2 : 1;
  const int T0 = f < 3 ? 2 * (3 * j + f) : 48 + j;
  asm volatile("s_nop 4");
  if (f < 3) {
    c.vW1 = (l >> 3) * 3584 + 64 * T0 + swz; c.vW1b = c.vW1 + 8 * 3584;
    c.la0 = (8 * h + q4) * 128 + cpart; c.la1 = (c.la0 ^ 64) + c.ring; c.la0 += c.ring;
    __builtin_amdgcn_s_setprio(FPRIO);
#pragma unroll
    for (int i = 0; i < EARLY; i++) f1_issue_w1<2>(c, c.ring + i * 2048, i);
    wait_vm<2 * EARLY>();
  } else {
    c.vW1 = (l >> 2) * 3584 + 64 * T0 + 16 * (l & 3); c.vW1b = c.vW1;
    c.la0 = (8 * h + q4) * 64 + 32 * g1 + 8 * (l & 3) + c.ring; c.la1 = c.la0;
    wait_vm<0>();
  }
  asm volatile("" : "+v"(epoch));
  const unsigned tag = __builtin_amdgcn_readfirstlane(epoch) + 1u;
  __builtin_amdgcn_s_setprio(FPRIO);
  grp_arrive(lds + CX);
  if (f < 3) {
#pragma unroll
    for (int i = EARLY; i < 7; i++) f1_issue_w1<2>(c, c.ring + i * 2048, i);
  }
  cnt_wait(lds + CX, 4);
  if (f == 3) {
    if (j < 4) {
      __builtin_amdgcn_s_setprio(3);
      const int ts = j & 1, t32 = 2 * g64 + ts;
      const unsigned wb = L_WKV + (8 * h + q4) * 56 + 32 * g1 + 8 * (l & 3);
      const unsigned xo = ts * 28672;
      f32x16 a; zero16(a);
      if (j < 2) {
        proj_chain<false, 896, 224, 0, 4>(a, lds, wb, c.xl + xo);
        float k[16]; float ss = 0.f;
#pragma unroll
        for (int r = 0; r < 16; r++) { k[r] = a[r]; if (r >= 12 && h) k[r] = 0.f; ss = __builtin_fmaf(k[r], k[r], ss); }
        u32x4 f0 = {pk(k[0], k[1]), pk(k[2], k[3]), pk(k[4], k[5]), pk(k[6], k[7])};
        u32x4 f1 = {pk(k[8], k[9]), pk(k[10], k[11]), pk(k[12], k[13]), pk(k[14], k[15])};
        st16_sc1(f0, sWS, WS_KV + t32 * 4096 + l * 16, 0); st16_sc1(f1, sWS, WS_KV + t32 * 4096 + 1024 + l * 16, 0);
        const float mx = wave_max_nn(half_sum(ss));
        wait_vm<0>();
        if (l < 32) __builtin_amdgcn_raw_buffer_store_b64(u32x2{__float_as_uint(mx), tag}, sFL, FL_KV + l * 1024 + t32 * 16, 0, AUX_SC1);
      } else {
        proj_chain<true, 896, 224, 0, 4>(a, lds, wb, c.xl + xo);
        float v[16]; float mx = 0.f;
        const int d = l & 31;
#pragma unroll
        for (int r = 0; r < 16; r++) { v[r] = d < 28 ? a[r] : 0.f; mx = fmaxf(mx, fabsf(v[r])); if (d == 28) v[r] = 1.0f; }
        u32x4 f0 = {pk(v[0], v[1]), pk(v[2], v[3]), pk(v[4], v[5]), pk(v[6], v[7])};
        u32x4 f1 = {pk(v[8], v[9]), pk(v[10], v[11]), pk(v[12], v[13]), pk(v[14], v[15])};
        st16_sc1(f0, sWS, WS_KV + t32 * 4096 + 2048 + l * 16, 0); st16_sc1(f1, sWS, WS_KV + t32 * 4096 + 3072 + l * 16, 0);
        mx = wave_max_nn(mx);
        wait_vm<0>();
        if (l < 32) __builtin_amdgcn_raw_buffer_store_b64(u32x2{__float_as_uint(mx), tag}, sFL, FL_KV + l * 1024 + t32 * 16 + 8, 0, AUX_SC1);
      }
      __builtin_amdgcn_s_setprio(FPRIO);
    }
    lgkm0();
#pragma unroll
    for (int i = 0; i < 7; i++) f1_issue_w1<1>(c, c.ring + i * 2048, i);
  }
#pragma unroll
  for (int n = 0; n < 2; n++)
#pragma unroll
    for (int m = 0; m < 4; m++) {
      u32x2 v = *(LAS u32x2*)(lds + L_B1 + f * 256 + (32 * n + 8 * m + 4 * h) * 2);
#pragma unroll
      for (int t = 0; t < 2; t++) {
        acc[n][t][4 * m] = bflo(v[0]); acc[n][t][4 * m + 1] = bfhi(v[0]); acc[n][t][4 * m + 2] = bflo(v[1]); acc[n][t][4 * m + 3] = bfhi(v[1]);
      }
    }
  if (f < 3) ffn1<2>(c, acc, ws); else ffn1<1>(c, acc, ws);
  unsigned pf2 = 0;
  {
    const unsigned wi = sl * 4 + f;
    const unsigned i0 = wi * 64 + l, b0 = i0 / 112, i1 = 8192 + wi * 10 + l, b1 = i1 / 112;
    const unsigned l0 = 3136 * (b0 & 3) + 112 * (7 + (b0 >> 2)) + (i0 - b0 * 112), l1 = 3136 * (b1 & 3) + 112 * (7 + (b1 >> 2)) + (i1 - b1 * 112);
    asm volatile("s_nop 4\n\tbuffer_load_dword %0, %1, %2, 0 offen" : "+v"(pf2) : "v"(l0 * 128), "s"(c.sW2) : "memory");
    asm volatile("buffer_load_dword %0, %1, %2, 0 offen" : "+v"(pf2) : "v"(l < 10 && i1 < 9408 ? l1 * 128 : 0x40000000u), "s"(c.sW2) : "memory");
    asm volatile("buffer_load_dword %0, %1, %2, 0 offen" : "+v"(pf2) : "v"(l < 25 ? (wi * 25 + l) * 128 : 0x40000000u), "s"(c.sWO) : "memory");
  }
  lgkm0();
  grp_arrive(lds + C3);
  __builtin_amdgcn_s_setprio(GPRIO);
  {
    f32x2 c1 = {-2.3022082f, -2.3022082f}, c2 = {-2.3022082f * 0.044715f, -2.3022082f * 0.044715f};
    asm volatile("" : "+v"(c1), "+v"(c2));
#pragma unroll
    for (int n = 0; n < 2; n++) {
      if (n < nsub) {
#pragma unroll
        for (int t = 0; t < 2; t++) {
          const u32x4 f0 = gelu8(acc[n][t], 0, c1, c2), f1 = gelu8(acc[n][t], 8, c1, c2);
          const unsigned so = WS_H + ((2 * g64 + t) * 112 + 2 * (T0 + n)) * 1024;
          st16_sc1(f0, sWS, l * 16, so); st16_sc1(f1, sWS, l * 16, so + 1024);
        }
      }
    }
  }
  asm volatile("s_waitcnt vmcnt(0)" : "+v"(pf2) :: "memory");
  h_arrive(lds, sFL, g64, j, l, tag);
  if (j == 7) {
    if (g64 == 0 && f == 0) {
      poll4(sFL, FL_HG + l * 4, tag); poll4(sFL, FL_HG + 256 + l * 4, tag); poll4(sFL, FL_HG + 512 + l * 4, tag); poll4(sFL, FL_HG + 768 + l * 4, tag);
      if (l == 0) __builtin_amdgcn_raw_buffer_store_b32(tag, sFL, 0, 0, AUX_SC1);
    }
    return;
  }
  c.la0 = ((4 * h + q4) * 128 + cpart) ^ (64 * (f >> 1)); c.la1 = (c.la0 ^ 64) + c.ring; c.la0 += c.ring;
  __builtin_amdgcn_s_setprio(F2PRIO);
  poll4(sFL, FL_H + (g64 * 8 + j) * 128 + (l & 7) * 4, tag);
  float og0, og1;
  ffn2_tail(c, acc, g64, l, sWS, og0, og1, ws);
  lgkm0();
#pragma unroll
  for (int qd = 1; qd < 4; qd++) {
    const f32x16& a = acc[qd >> 1][qd & 1];
#pragma unroll
    for (int g = 0; g < 4; g++)
      *(LAS f32x4*)(lds + c.ring + (qd - 1) * 4096 + g * 1024 + l * 16) = f32x4{a[4 * g], a[4 * g + 1], a[4 * g + 2], a[4 * g + 3]};
  }
  grp_arrive(lds + CF);
  f32x16 t = acc[0][0];
#pragma unroll
  for (int m = 0; m < 4; m++) {
    u32x2 v = *(LAS u32x2*)(lds + L_B2 + (32 * (f >> 1) + 8 * m + 4 * h) * 2);
    t[4 * m] = __builtin_fmaf(og0, bflo(v[0]), t[4 * m]); t[4 * m + 1] = __builtin_fmaf(og0, bfhi(v[0]), t[4 * m + 1]);
    t[4 * m + 2] = __builtin_fmaf(og0, bflo(v[1]), t[4 * m + 2]); t[4 * m + 3] = __builtin_fmaf(og0, bfhi(v[1]), t[4 * m + 3]);
  }
  cnt_spin(lds + CF, 4);
  add3(t, lds + ring_off(f ^ 1) + l * 16, lds + ring_off(f ^ 2) + 4096 + l * 16, lds + ring_off(f ^ 3) + 8192 + l * 16);
  {
    unsigned p[8];
#pragma unroll
    for (int i = 0; i < 8; i++) p[i] = pk(t[2 * i], t[2 * i + 1]);
    const srd_t sOut = mk_srd(P.out, S * D * 2);
    const unsigned row = (64 * g64 + 32 * (f & 1) + (l & 31)) * 896 + 128 * j + 64 * (f >> 1) + 16 * h;
#pragma unroll
    for (int m = 0; m < 2; m++) {
      auto s0 = __builtin_amdgcn_permlane32_swap(p[4 * m], p[4 * m + 2], false, false);
      auto s1 = __builtin_amdgcn_permlane32_swap(p[4 * m + 1], p[4 * m + 3], false, false);
      u32x4 v = {s0[0], s1[0], s0[1], s1[1]};
      __builtin_amdgcn_raw_buffer_store_b128(v, sOut, row + 32 * m, 0, 0);
    }
  }
}

__device__ __forceinline__ void a_wave(const Ptrs& P, ldsp lds, unsigned ldsb, int g64, int j, int a, int l, unsigned epoch, unsigned gwv,
                                       u32x4 (&wq1)[7], srd_t sWS, srd_t sFL) {
  const int h = l >> 5;
  char* ws = P.ws;
  const unsigned gad = L_WQ + (56 * a + 2 * (l & 31)) * 128 + 16 * (7 ^ ((l & 1) << 2));
  bf16x8 xf[28];
  {
    const unsigned xl = L_X + (a & 1) * 28672 + ((l & 31) >> 3) * 7168 + (l & 7) * 128 + 16 * (h ^ (((l & 31) >> 1) & 7));
    cnt_wait(lds + CX, 4);
#pragma unroll
    for (int i = 0; i < 28; i++) xf[i] = rd128(lds + (xl ^ (32u * (i & 3))) + (i >> 2) * 1024);
#pragma unroll
    for (int i = 0; i < 28; i++) PIN(xf[i]);
  }
  wait_vm<7>();
  asm volatile("" : "+v"(gwv), "+v"(epoch));
  const unsigned tag = __builtin_amdgcn_readfirstlane(epoch) + 1u;
  if (l < 28) {
    *(LAS unsigned short*)(lds + gad) = (unsigned short)gwv;
    *(LAS unsigned short*)(lds + gad + 128) = (unsigned short)(gwv >> 16);
  }
  grp_arrive(lds + C0);
  const int hb = a >> 1, ts = a & 1;
  const unsigned q4 = (l & 15) >> 2, g1 = (l >> 4) & 1;
  const unsigned u8 = 7 * hb + 4 * g1 + (l & 3);
  const unsigned wb = L_WQ + (8 * h + q4) * 128 + 16 * ((u8 >> 1) ^ (((q4 >> 1) & 1) << 2)) + 8 * (u8 & 1);
  cnt_wait(lds + C0, 4);
  f32x16 acc; zero16(acc);
  bf16x8 wf[14];
#pragma unroll
  for (int i = 0; i < 14; i++) wf[i] = rdtr(lds + wb + i * 2048, lds + wb + i * 2048 + 512);
  grp_arrive(lds + C1);
  SB();
#pragma unroll
  for (int i = 0; i < 14; i++) acc = mfma(wf[i], xf[i], acc);
  PIN(acc);
  SB();
  asm volatile("s_waitcnt vmcnt(0)" : "+v"(wq1[0]), "+v"(wq1[1]), "+v"(wq1[2]), "+v"(wq1[3]), "+v"(wq1[4]), "+v"(wq1[5]), "+v"(wq1[6]) :: "memory");
  cnt_spin(lds + C1, 4);
#pragma unroll
  for (int i = 0; i < 7; i++) *(LAS u32x4*)(lds + L_WQ + (56 * a + 8 * i) * 128 + l * 16) = wq1[i];
  if (l >= 32 && l < 60) {
    *(LAS unsigned short*)(lds + gad) = (unsigned short)gwv;
    *(LAS unsigned short*)(lds + gad + 128) = (unsigned short)(gwv >> 16);
  }
  grp_arrive(lds + C1);
  h_arrive(lds, sFL, g64, j, l, tag);
  const unsigned kvf = FL_KV + g64 * 1024 + (32 * a + (l & 31)) * 8;
  cnt_spin(lds + C1, 8);
#pragma unroll
  for (int i = 0; i < 14; i++) wf[i] = rdtr(lds + wb + i * 2048, lds + wb + i * 2048 + 512);
  SB();
#pragma unroll
  for (int i = 0; i < 14; i++) acc = mfma(wf[i], xf[14 + i], acc);
  PIN(acc);
  SB();
  u32x2 fl;
  asm volatile("buffer_load_dwordx2 %0, %1, %2, 0 offen sc1" : "=v"(fl) : "v"(kvf), "s"(sFL) : "memory");
  {
    const float qs = 0.18898223650461362f * 1.4426950408889634f;
    const float gt = __builtin_amdgcn_rcpf(1.0f + __builtin_amdgcn_exp2f(acc[12] * -1.4426950408889634f));
    float q[16]; float ss = 0.f;
#pragma unroll
    for (int r = 0; r < 16; r++) { q[r] = acc[r] * qs; if (r >= 12 && h) q[r] = 0.f; ss = __builtin_fmaf(q[r], q[r], ss); }
    u32x4 f0 = {pk(q[0], q[1]), pk(q[2], q[3]), pk(q[4], q[5]), pk(q[6], q[7])};
    u32x4 f1 = {pk(q[8], q[9]), pk(q[10], q[11]), pk(q[12], q[13]), pk(q[14], q[15])};
    const float mx = wave_max_nn(half_sum(ss));
    *(LAS u32x4*)(lds + L_QX + a * 2048 + l * 16) = f0; *(LAS u32x4*)(lds + L_QX + a * 2048 + 1024 + l * 16) = f1;
    if (l == 0) *(LAS float*)(lds + L_QN + a * 4) = mx;
    if (hb && h) *(LAS float*)(lds + L_GATE + (32 * ts + (l & 31)) * 4) = gt;
  }
  grp_arrive(lds + C2);
  asm volatile("s_waitcnt vmcnt(0)" : "+v"(fl) :: "memory");
  if (__builtin_amdgcn_ballot_w64(fl[1] != tag) != 0) fl = poll8(sFL, kvf, tag);
  const srd_t sKV = mk_srd(ws + WS_KV, 64 * 4096);
  KVT kv[4];
  asm volatile("s_nop 4");
  kv_issue(kv[0], l * 16, sKV, a * 16 * 4096);
  const float st = __uint_as_float(fl[0]);
  const float kn = wave_max_nn((l & 1) ? 0.f : st), vm = wave_max_nn((l & 1) ? st : 0.f);
  cnt_wait(lds + C2, 4);
  bf16x8 q[4][2];
#pragma unroll
  for (int u = 0; u < 4; u++) {
    const unsigned qa = L_QX + ((a + u) & 3) * 2048 + l * 16;
    q[u][0] = rd128(lds + qa); q[u][1] = rd128(lds + qa + 1024);
  }
  const f32x4 qn4 = *(LAS f32x4*)(lds + L_QN);
  const float gate = *(LAS float*)(lds + L_GATE + (32 * (a & 1) + (l & 31)) * 4);
  kv_issue(kv[1], l * 16, sKV, a * 16 * 4096 + 4096);
  kv_issue(kv[2], l * 16, sKV, a * 16 * 4096 + 8192);
  const float qn = fmaxf(fmaxf(qn4[0], qn4[1]), fmaxf(qn4[2], qn4[3]));
  const float bound = __builtin_sqrtf(qn * kn) * 1.02f + fabsf(__builtin_amdgcn_logf(vm));
  f32x16 o[4];
#pragma unroll
  for (int u = 0; u < 4; u++) zero16(o[u]);
  float m[4] = {0.f, 0.f, 0.f, 0.f};
  const bool fast = __builtin_amdgcn_readfirstlane(bound <= 100.0f);
  __builtin_amdgcn_s_setprio(APRIO2);
  if (fast) attn_fast(o, q, kv, sKV, a * 16 * 4096, l);
  else { kv_wait<0>(kv[0]); kv_wait<0>(kv[1]); kv_wait<0>(kv[2]); attn_safe(o, m, q, sKV, a * 16 * 4096, l); }
  cnt_spin(lds + C3, 4);
#pragma unroll
  for (int u = 1; u < 4; u++) {
    const unsigned dst = L_RED + (((a + u) & 3) * 3 + (u - 1)) * 4096 + l * 16;
#pragma unroll
    for (int g = 0; g < 4; g++) *(LAS f32x4*)(lds + dst + g * 1024) = f32x4{o[u][4 * g], o[u][4 * g + 1], o[u][4 * g + 2], o[u][4 * g + 3]};
  }
  if (!fast) {
#pragma unroll
    for (int u = 0; u < 4; u++) *(LAS float*)(lds + L_M + (a * 4 + ((a + u) & 3)) * 256 + l * 4) = m[u];
  }
  grp_arrive(lds + CA);
  f32x16 t = o[0];
  cnt_spin(lds + CA, 4);
  if (fast) {
    ldsp pr = lds + L_RED + a * 12288 + l * 16;
    add3(t, pr, pr + 4096, pr + 8192);
  } else {
    float mw[4];
#pragma unroll
    for (int u = 0; u < 4; u++) mw[u] = *(LAS float*)(lds + L_M + (((a - u) & 3) * 4 + a) * 256 + l * 4);
    const float M = fmaxf(fmaxf(mw[0], mw[1]), fmaxf(mw[2], mw[3]));
    const float so = __builtin_amdgcn_exp2f(mw[0] - M);
#pragma unroll
    for (int r = 0; r < 16; r++) t[r] *= so;
#pragma unroll
    for (int u = 1; u < 4; u++) {
      const float sc = __builtin_amdgcn_exp2f(mw[u] - M);
#pragma unroll
      for (int g = 0; g < 4; g++) {
        const f32x4 v = *(LAS f32x4*)(lds + L_RED + (a * 3 + (u - 1)) * 4096 + g * 1024 + l * 16);
#pragma unroll
        for (int e = 0; e < 4; e++) t[4 * g + e] = __builtin_fmaf(v[e], sc, t[4 * g + e]);
      }
    }
  }
  const float lsum = half_sum(h ? t[12] : 0.f);
  const float sc = gate * __builtin_amdgcn_rcpf(lsum);
  float y[16];
#pragma unroll
  for (int r = 0; r < 16; r++) { y[r] = t[r] * sc; if (r >= 12 && h) y[r] = 0.f; }
  u32x4 f0 = {pk(y[0], y[1]), pk(y[2], y[3]), pk(y[4], y[5]), pk(y[6], y[7])};
  u32x4 f1 = {pk(y[8], y[9]), pk(y[10], y[11]), pk(y[12], y[13]), pk(y[14], y[15])};
  const unsigned oo = WS_O + ((2 * g64 + (a & 1)) * 32 + 2 * (2 * j + (a >> 1))) * 1024;
  st16_sc1(f0, sWS, l * 16, oo); st16_sc1(f1, sWS, l * 16, oo + 1024);
}

__global__ void __launch_bounds__(512, 2) fused_kernel(const char* __restrict__ x, const char* __restrict__ wq, const char* __restrict__ wk,
                                                       const char* __restrict__ wv, const char* __restrict__ wo, const char* __restrict__ w1,
                                                       const char* __restrict__ b1, const char* __restrict__ w2, const char* __restrict__ b2,
                                                       const char* __restrict__ gw, char* __restrict__ ws, char* __restrict__ out) {
  extern __shared__ __attribute__((aligned(16))) char smem[];
  asm volatile("" :: "s"(x), "s"(wq), "s"(wk), "s"(wv), "s"(wo), "s"(w1), "s"(b1), "s"(w2), "s"(b2), "s"(gw), "s"(ws), "s"(out));
  ldsp lds = (ldsp)smem;
  const unsigned ldsb = (unsigned)(size_t)lds;
  const int b = blockIdx.x, xcd = b & 7, sl = b >> 3;
  const int g64 = 4 * xcd + (sl >> 3), j = sl & 7;
  const int w = __builtin_amdgcn_readfirstlane(threadIdx.x >> 6), l = threadIdx.x & 63;
  const srd_t sWS = mk_srd(ws, (unsigned)WS_B), sFL = mk_srd(ws + WS_FL, FL_SIZE);
  unsigned epoch;
  unsigned gwv = 0;
  u32x4 wq1[7];
  if (w >= 4) {
    __builtin_amdgcn_s_setprio(PPRIO);
    const int f = w - 4;
    const srd_t sX = mk_srd(x, S * D * 2);
    const unsigned tok = 16 * f + (l >> 3);
    const unsigned vx0 = (64 * g64 + tok) * 896 + 16 * ((l & 7) ^ ((tok >> 1) & 7));
    const unsigned vx1 = (64 * g64 + tok + 8) * 896 + 16 * ((l & 7) ^ (((tok + 8) >> 1) & 7));
    asm volatile("s_nop 4");
#pragma unroll
    for (int ch = 0; ch < 7; ch++) {
      dma16(ldsb + L_X + (2 * f * 7 + ch) * 1024, vx0, sX, ch * 128);
      dma16(ldsb + L_X + ((2 * f + 1) * 7 + ch) * 1024, vx1, sX, ch * 128);
    }
    if (j < 4) {
      const srd_t sK = mk_srd(j < 2 ? wk : wv, D * DH * 2);
      asm volatile("s_nop 4");
#pragma unroll
      for (int i = 0; i < 6; i++) dma16(ldsb + L_WKV + (f + 4 * i) * 1024, l * 16, sK, (f + 4 * i) * 1024);
      if (f == 0 && l < 32) dma16(ldsb + L_WKV + 24 * 1024, l * 16, sK, 24 * 1024);
    }
    asm volatile("s_nop 4\n\tbuffer_load_dword %0, off, %1, 0 sc1" : "=v"(epoch) : "s"(sFL) : "memory");
    const int T0 = f < 3 ? 2 * (3 * j + f) : 48 + j;
    asm volatile("s_nop 4");
    if (f < 3 || l < 16) dma4(ldsb + L_B1 + f * 256, l * 4, mk_srd(b1, F * 2), 64 * T0);
    if (f == 0 && l < 32) dma4(ldsb + L_B2, l * 4, mk_srd(b2, D * 2), 128 * j);
  } else {
    const srd_t sQ = mk_srd(wq, D * D * 2);
    const unsigned vq = (56 * w + (l >> 3)) * 896 + 112 * j + 16 * ((l & 7) ^ (((l >> 4) & 1) << 2));
    __builtin_amdgcn_s_setprio(APRIO);
    const srd_t sG = mk_srd(gw, D * 2);
    asm volatile("s_nop 4");
#pragma unroll
    for (int i = 0; i < 7; i++) dma16(ldsb + L_WQ + (56 * w + 8 * i) * 128, vq, sQ, i * 8 * 896);
    asm volatile("s_nop 4\n\tbuffer_load_dword %0, off, %1, 0 sc1" : "=v"(epoch) : "s"(sFL) : "memory");
    asm volatile("s_nop 4\n\tbuffer_load_dword %0, %1, %2, 0 offen" : "=v"(gwv) : "v"((224 * (l >> 5) + 56 * w + 2 * (l & 31)) * 2), "s"(sG) : "memory");
    {
      const unsigned oo = WS_O + ((2 * g64 + (w & 1)) * 32 + 2 * (2 * j + (w >> 1))) * 1024;
      const u32x4 mk = {SENT, SENT, SENT, SENT};
      st16_sc1(mk, sWS, l * 16, oo); st16_sc1(mk, sWS, l * 16, oo + 1024);
    }
    __builtin_amdgcn_s_setprio(WQ1PRIO);
#pragma unroll
    for (int i = 0; i < 7; i++)
      asm volatile("buffer_load_dwordx4 %0, %1, %2, %3 offen" : "=v"(wq1[i]) : "v"(vq), "s"(sQ), "s"((224 + i * 8) * 896) : "memory");
    __builtin_amdgcn_s_setprio(APRIO);
  }
  if (threadIdx.x < 16) *(LAS unsigned*)(lds + L_CNT + threadIdx.x * 4) = 0;
  lgkm0();
  bar();
  Ptrs P = {x, wq, wk, wv, wo, w1, b1, w2, b2, gw, ws, out};
  if (w < 4) {
    a_wave(P, lds, ldsb, g64, j, w, l, epoch, gwv, wq1, sWS, sFL);
  } else f_wave(P, lds, ldsb, g64, j, sl, w - 4, l, epoch, sWS, sFL);
}

extern "C" void kernel_launch(void* const* d_in, const int* in_sizes, int n_in, void* d_out, int out_size, void* d_ws, size_t ws_size,
                              hipStream_t stream) {
  (void)hipFuncSetAttribute((const void*)fused_kernel, hipFuncAttributeMaxDynamicSharedMemorySize, L_TOTAL);
  fused_kernel<<<256, 512, L_TOTAL, stream>>>((const char*)d_in[0], (const char*)d_in[1], (const char*)d_in[2], (const char*)d_in[3],
                                              (const char*)d_in[4], (const char*)d_in[6], (const char*)d_in[7], (const char*)d_in[8],
                                              (const char*)d_in[9], (const char*)d_in[10], (char*)d_ws, (char*)d_out);
}
```
